```python
import torch

def _softmax(x, axis=-1):
    return torch.softmax(x, dim=axis)
def _gelu(x):
    return torch.nn.functional.gelu(x, approximate="tanh")
def _silu(x):
    return torch.nn.functional.silu(x)
def _sigmoid(x):
    return torch.sigmoid(x)
def _layernorm(x, axis=-1, eps=1e-5):
    m = x.mean(dim=axis, keepdim=True)
    v = x.var(dim=axis, keepdim=True, unbiased=False)
    return (x - m) / torch.sqrt(v + eps)
def _scatter_add(tgt, idx, src, axis=0):
    return tgt.clone().index_add_(axis, idx, src)
def _segment_sum(src, seg, n):
    out = torch.zeros((n,) + src.shape[1:], dtype=src.dtype)
    return out.index_add_(0, seg, src)
def _pad(x, pw):
    flat = tuple(v for pair in reversed(pw) for v in pair)
    return torch.nn.functional.pad(x, flat)

kernel_name = 'interleave_layers__sliding_block_cached__mixer_channel_mlp__decode'

def setup_inputs():

    _w = (lambda a: (lambda t: t.to(torch.bfloat16) if t.is_floating_point() else t)(torch.as_tensor(a)))
    def _A_setup():
        B, Sq, D = 1, 1, 448
        Hq, Hkv, Sk, Dh = 16, 8, 65536, 256
        assert Hq % Hkv == 0, (Hq, Hkv)
        return {
            "x": _w((lambda *s: torch.randn(*s, dtype=torch.bfloat16) * 0.05)(B, Sq, D)),
            "k_cache": _w((lambda *s: torch.randn(*s, dtype=torch.bfloat16) * 3.2)(B, Hkv, Sk, Dh)),
            "v_cache": _w((lambda *s: torch.randn(*s, dtype=torch.bfloat16) * 3.2)(B, Hkv, Sk, Dh)),
            "wq": _w((lambda *s: torch.randn(*s, dtype=torch.bfloat16) * 3.2)(D, Hq * Dh) * (D ** -0.5)),
            "wo": _w((lambda *s: torch.randn(*s, dtype=torch.bfloat16) * 3.2)(Hq * Dh, D) * ((Hq * Dh) ** -0.5)),
        }
    _A_in = _A_setup()

    def _B_setup():
        B, S, D, F = 1, 1, 448, 1792
        return {
            "x": _w((lambda *s: torch.randn(*s, dtype=torch.bfloat16) * 0.05)(B, S, D)),
            "w1": _w((lambda *s: torch.randn(*s, dtype=torch.bfloat16) * 3.2)(D, F) * (D ** -0.5)),
            "b1": _w((lambda *s: torch.randn(*s, dtype=torch.bfloat16) * 3.2)(F)),
            "w2": _w((lambda *s: torch.randn(*s, dtype=torch.bfloat16) * 3.2)(F, D) * (F ** -0.5)),
            "b2": _w((lambda *s: torch.randn(*s, dtype=torch.bfloat16) * 3.2)(D)),
        }
    _B_in = _B_setup()

    return {
        "A_x": _A_in["x"],
        "A_k_cache": _A_in["k_cache"],
        "A_v_cache": _A_in["v_cache"],
        "A_wq": _A_in["wq"],
        "A_wo": _A_in["wo"],
        "B_x": _B_in["x"],
        "B_w1": _B_in["w1"],
        "B_b1": _B_in["b1"],
        "B_w2": _B_in["w2"],
        "B_b2": _B_in["b2"],
    }

def reference(A_x, A_k_cache, A_v_cache, A_wq, A_wo, B_x, B_w1, B_b1, B_w2, B_b2):
    def _A_ref(x, k_cache, v_cache, wq, wo):
        B, Sq, D = x.shape
        Hq, Dh = 16, 256
        Hkv, Sk = k_cache.shape[1], k_cache.shape[2]
        W = min(4096, max(256, Sk // 4))
        rep = Hq // Hkv
        q = (lambda x, *d: x.permute(*d))((x @ wq).reshape(B, Sq, Hq, Dh), 0, 2, 1, 3)
        k = (lambda x, n, axis: torch.repeat_interleave(x, n, dim=axis))(k_cache[:, :, Sk - W:, :], rep, axis=1)
        v = (lambda x, n, axis: torch.repeat_interleave(x, n, dim=axis))(v_cache[:, :, Sk - W:, :], rep, axis=1)
        scores = torch.einsum("bhqd,bhkd->bhqk", q, k) * (Dh ** -0.5)
        p = _softmax(scores, axis=-1)
        o = torch.einsum("bhqk,bhkd->bhqd", p, v)
        return (lambda x, *d: x.permute(*d))(o, 0, 2, 1, 3).reshape(B, Sq, Hq * Dh) @ wo

    def _B_ref(x, w1, b1, w2, b2):
        y = _layernorm(x, axis=-1)
        h = _gelu(torch.einsum("bsd,df->bsf", y, w1) + b1)
        return torch.einsum("bsf,fd->bsd", h, w2) + b2

    x = A_x
    x = x + _A_ref(_layernorm(x, axis=-1), A_k_cache, A_v_cache, A_wq, A_wo)
    x = x + _B_ref(_layernorm(x, axis=-1), B_w1, B_b1, B_w2, B_b2)
    x = x + _A_ref(_layernorm(x, axis=-1), A_k_cache, A_v_cache, A_wq, A_wo)
    x = x + _B_ref(_layernorm(x, axis=-1), B_w1, B_b1, B_w2, B_b2)
    return x
```

```cpp
#include <hip/hip_runtime.h>
#include <hip/hip_bf16.h>
#include <stdint.h>

typedef unsigned short u16;
__device__ __forceinline__ float bf2f(u16 v) { return __uint_as_float(((unsigned)v) << 16); }
__device__ __forceinline__ u16 f2bf(float f) { unsigned u = __float_as_uint(f); return (u16)((u + 0x7fffu + ((u >> 16) & 1u)) >> 16); }
__device__ __forceinline__ float rbf(float f) { return bf2f(f2bf(f)); }

constexpr int D = 448, HQ = 16, HKV = 8, SK = 65536, DH = 256, W = 4096, F = 1792;

__device__ void block_ln(const float* in, float* out, float* red) {
    int t = threadIdx.x;
    float s = 0.f;
    for (int i = t; i < D; i += blockDim.x) s += in[i];
    red[t] = s; __syncthreads();
    for (int o = blockDim.x / 2; o > 0; o >>= 1) { if (t < o) red[t] += red[t + o]; __syncthreads(); }
    float mean = red[0] / D; __syncthreads();
    float s2 = 0.f;
    for (int i = t; i < D; i += blockDim.x) { float d = in[i] - mean; s2 += d * d; }
    red[t] = s2; __syncthreads();
    for (int o = blockDim.x / 2; o > 0; o >>= 1) { if (t < o) red[t] += red[t + o]; __syncthreads(); }
    float var = red[0] / D; __syncthreads();
    float mb = rbf(mean), vb = rbf(var);
    float den = rbf(sqrtf(rbf(vb + 1e-5f)));
    for (int i = t; i < D; i += blockDim.x) out[i] = rbf(rbf(in[i] - mb) / den);
    __syncthreads();
}

__global__ void k_init(const u16* x, float* xs) { int t = threadIdx.x; if (t < D) xs[t] = bf2f(x[t]); }

__global__ void k_qproj(const float* xs, const u16* wq, float* q) {
    __shared__ float xin[D], y[D], red[256];
    for (int i = threadIdx.x; i < D; i += 256) xin[i] = xs[i];
    __syncthreads();
    block_ln(xin, y, red);
    int c = blockIdx.x * 256 + threadIdx.x;
    float acc = 0.f;
    for (int k = 0; k < D; ++k) acc += y[k] * bf2f(wq[(size_t)k * 4096 + c]);
    q[c] = rbf(acc);
}

__global__ void k_scores(const float* q, const u16* kc, float* sc) {
    __shared__ float qs[DH];
    int h = blockIdx.x; int key = blockIdx.y * 256 + threadIdx.x;
    qs[threadIdx.x] = q[h * DH + threadIdx.x];
    __syncthreads();
    const u16* kr = kc + ((size_t)(h / 2) * SK + (SK - W) + key) * DH;
    float acc = 0.f;
    for (int d = 0; d < DH; ++d) acc += qs[d] * bf2f(kr[d]);
    sc[h * W + key] = rbf(acc) * 0.0625f;
}

__global__ void k_pv(const float* sc, const u16* vc, float* o) {
    __shared__ float p[W]; __shared__ float red[256];
    int h = blockIdx.x, t = threadIdx.x;
    float m = -INFINITY;
    for (int i = t; i < W; i += 256) m = fmaxf(m, sc[h * W + i]);
    red[t] = m; __syncthreads();
    for (int s = 128; s > 0; s >>= 1) { if (t < s) red[t] = fmaxf(red[t], red[t + s]); __syncthreads(); }
    m = red[0]; __syncthreads();
    float l = 0.f;
    for (int i = t; i < W; i += 256) { float e = expf(sc[h * W + i] - m); p[i] = e; l += e; }
    red[t] = l; __syncthreads();
    for (int s = 128; s > 0; s >>= 1) { if (t < s) red[t] += red[t + s]; __syncthreads(); }
    l = red[0]; __syncthreads();
    for (int i = t; i < W; i += 256) p[i] = p[i] / l;
    __syncthreads();
    const u16* vr = vc + ((size_t)(h / 2) * SK + (SK - W)) * DH + t;
    float acc = 0.f;
    for (int k = 0; k < W; ++k) acc += p[k] * bf2f(vr[(size_t)k * DH]);
    o[h * DH + t] = rbf(acc);
}

__global__ void k_wo(const float* o, const u16* wo, float* xs) {
    int c = blockIdx.x * 64 + threadIdx.x;
    float acc = 0.f;
    for (int k = 0; k < HQ * DH; ++k) acc += o[k] * bf2f(wo[(size_t)k * D + c]);
    xs[c] = rbf(xs[c] + rbf(acc));
}

__device__ __forceinline__ float gelu_tanh(float x) {
    const float k0 = 0.7978845608028654f, k1 = 0.044715f;
    float inner = k0 * (x + k1 * x * x * x);
    return 0.5f * x * (1.f + tanhf(inner));
}

__global__ void k_w1(const float* xs, const u16* w1, const u16* b1, float* hout) {
    __shared__ float xin[D], y[D], y2[D], red[256];
    for (int i = threadIdx.x; i < D; i += 256) xin[i] = xs[i];
    __syncthreads();
    block_ln(xin, y, red);
    block_ln(y, y2, red);
    int c = blockIdx.x * 256 + threadIdx.x;
    float acc = 0.f;
    for (int k = 0; k < D; ++k) acc += y2[k] * bf2f(w1[(size_t)k * F + c]);
    hout[c] = rbf(gelu_tanh(rbf(rbf(acc) + bf2f(b1[c]))));
}

__global__ void k_w2(const float* h, const u16* w2, const u16* b2, float* xs, u16* out) {
    int c = blockIdx.x * 64 + threadIdx.x;
    float acc = 0.f;
    for (int k = 0; k < F; ++k) acc += h[k] * bf2f(w2[(size_t)k * D + c]);
    float r = rbf(xs[c] + rbf(rbf(acc) + bf2f(b2[c])));
    xs[c] = r;
    out[c] = f2bf(r);
}

extern "C" void kernel_launch(void* const* d_in, const int* in_sizes, int n_in, void* d_out, int out_size, void* d_ws, size_t ws_size, hipStream_t stream) {
    const u16* x = (const u16*)d_in[0];
    const u16* kc = (const u16*)d_in[1];
    const u16* vc = (const u16*)d_in[2];
    const u16* wq = (const u16*)d_in[3];
    const u16* wo = (const u16*)d_in[4];
    const u16* w1 = (const u16*)d_in[6];
    const u16* b1 = (const u16*)d_in[7];
    const u16* w2 = (const u16*)d_in[8];
    const u16* b2 = (const u16*)d_in[9];
    float* ws = (float*)d_ws;
    float* xs = ws; float* q = ws + 1024; float* sc = q + 4096; float* o = sc + HQ * W; float* h = o + 4096;
    k_init<<<1, 512, 0, stream>>>(x, xs);
    for (int l = 0; l < 2; ++l) {
        k_qproj<<<16, 256, 0, stream>>>(xs, wq, q);
        k_scores<<<dim3(HQ, W / 256), 256, 0, stream>>>(q, kc, sc);
        k_pv<<<HQ, 256, 0, stream>>>(sc, vc, o);
        k_wo<<<7, 64, 0, stream>>>(o, wo, xs);
        k_w1<<<7, 256, 0, stream>>>(xs, w1, b1, h);
        k_w2<<<7, 64, 0, stream>>>(h, w2, b2, xs, (u16*)d_out);
    }
}
```

```cpp
#include <hip/hip_runtime.h>
#include <stdint.h>

typedef unsigned u32;
typedef unsigned long long u64;
typedef u32 v4u __attribute__((ext_vector_type(4)));
typedef u32 v2u __attribute__((ext_vector_type(2)));
typedef float v4f __attribute__((ext_vector_type(4)));
typedef float v2f __attribute__((ext_vector_type(2)));
typedef __bf16 v8bf __attribute__((ext_vector_type(8)));
typedef __bf16 v2bf __attribute__((ext_vector_type(2)));

#define DEV __device__ __forceinline__
constexpr u32 SENT = 0xFFFFFFFFu;

constexpr u32 O_EPOCH = 0, O_F = 1024, O_A = 4096, O_S = 40960, O_ML = 303104, O_PB = 311296,
              O_PC = 458752, O_H = 720896, O_X2 = 786432, O_END = 794624, O_DBG = 1u << 20;

#define N1 6
#define D_A 6
#define D_S 6
#define D_PB 6
#define D_PC 8
#define D_PC2 40
#define D_H 22
#define D_X1 40
#define D_X2 8
#define D_R 2

constexpr int L_SP = 0, L_PP = 1024, L_WP = 1536, L_QF = 1664, L_SF = 1668, L_PF = 1672, L_WF = 1673, L_XF = 1674,
              L_HF = 1675, L_X1 = 1680, L_TAG = 1696, L_SZ = 1712;

#define FENCE() asm volatile("" ::: "memory")

template <int C> DEV u32 dppu(u32 v) { return __builtin_amdgcn_update_dpp(0u, v, C, 0xF, 0xF, true); }
template <int C> DEV float dppf(float v) { return __uint_as_float(dppu<C>(__float_as_uint(v))); }
constexpr int XOR1 = 0xB1, XOR2 = 0x4E, HMIR = 0x141, MIR = 0x140, ROR4 = 0x124, ROR8 = 0x128;
constexpr int SHL2 = 0x102, SHL4 = 0x104, SHL6 = 0x106, SHL8 = 0x108, SHL12 = 0x10C, SHL1 = 0x101, SHL3 = 0x103;

DEV u32 cvtpk(float a, float b) { v2f f = {a, b}; v2bf h = __builtin_convertvector(f, v2bf); return __builtin_bit_cast(u32, h); }
DEV float blo(u32 p) { return __uint_as_float(p << 16); }
DEV float bhi(u32 p) { return __uint_as_float(p & 0xFFFF0000u); }
DEV float rbf(float a) { return blo(cvtpk(a, a)); }
DEV u32 plo(u32 a, u32 b) { return __builtin_amdgcn_perm(b, a, 0x05040100u); }
DEV u32 phi(u32 a, u32 b) { return __builtin_amdgcn_perm(b, a, 0x07060302u); }
DEV float dot2(u32 a, u32 b, float c) { return __builtin_amdgcn_fdot2_f32_bf16(__builtin_bit_cast(v2bf, a), __builtin_bit_cast(v2bf, b), c, false); }
DEV v4f mfma(v4u a, v4u b, v4f c) { return __builtin_amdgcn_mfma_f32_16x16x32_bf16(__builtin_bit_cast(v8bf, a), __builtin_bit_cast(v8bf, b), c, 0, 0, 0); }
DEV float tsum32(float a, float b) { auto r = __builtin_amdgcn_permlane32_swap(__float_as_uint(a), __float_as_uint(b), false, false); return __uint_as_float(r[0]) + __uint_as_float(r[1]); }
DEV float tsum16(float a, float b) { auto r = __builtin_amdgcn_permlane16_swap(__float_as_uint(a), __float_as_uint(b), false, false); return __uint_as_float(r[0]) + __uint_as_float(r[1]); }
DEV float tmax16(float a) { auto r = __builtin_amdgcn_permlane16_swap(__float_as_uint(a), __float_as_uint(a), false, false); return fmaxf(__uint_as_float(r[0]), __uint_as_float(r[1])); }
DEV float tmax32(float a) { auto r = __builtin_amdgcn_permlane32_swap(__float_as_uint(a), __float_as_uint(a), false, false); return fmaxf(__uint_as_float(r[0]), __uint_as_float(r[1])); }
DEV float rowsum(float v) { v += dppf<XOR1>(v); v += dppf<XOR2>(v); v += dppf<HMIR>(v); v += dppf<MIR>(v); return v; }
DEV float rowmax(float v) { v = fmaxf(v, dppf<XOR1>(v)); v = fmaxf(v, dppf<XOR2>(v)); v = fmaxf(v, dppf<HMIR>(v)); v = fmaxf(v, dppf<MIR>(v)); return v; }
DEV float rdl(float v, int l) { return __uint_as_float(__builtin_amdgcn_readlane(__float_as_uint(v), l)); }
DEV bool bad4(v4u v) { return max(max(v.x, v.y), max(v.z, v.w)) == SENT; }
DEV float ex2(float x) { return __builtin_amdgcn_exp2f(x); }

typedef __amdgpu_buffer_rsrc_t rsrc_t;
DEV rsrc_t mkr(const void* p, u32 bytes) { return __builtin_amdgcn_make_buffer_rsrc((void*)p, 0, bytes, 0x00020000); }
constexpr int SC1 = 16, NT = 2;
#define BULK_AUX 0
#define LD128(r, o, aux) __builtin_amdgcn_raw_buffer_load_b128(r, o, 0, aux)
#define LD64(r, o, aux) __builtin_amdgcn_raw_buffer_load_b64(r, o, 0, aux)
#define LD32(r, o, aux) __builtin_amdgcn_raw_buffer_load_b32(r, o, 0, aux)
#define LD16(r, o, aux) __builtin_amdgcn_raw_buffer_load_b16(r, o, 0, aux)
#define LD128S(r, o, s, aux) __builtin_amdgcn_raw_buffer_load_b128(r, o, s, aux)
#define LD64S(r, o, s, aux) __builtin_amdgcn_raw_buffer_load_b64(r, o, s, aux)
#define LD32S(r, o, s, aux) __builtin_amdgcn_raw_buffer_load_b32(r, o, s, aux)
#define ST128(d, r, o, aux) __builtin_amdgcn_raw_buffer_store_b128(d, r, o, 0, aux)
#define ST64(d, r, o, aux) __builtin_amdgcn_raw_buffer_store_b64(d, r, o, 0, aux)

#define TS(id) do {} while (0)
#define CNT(id, v) do {} while (0)

#define LDSP __attribute__((address_space(3)))
typedef LDSP u32* lp;
DEV u32 lds_rd(lp p) { FENCE(); return *p; }
DEV v4u lds_rd4(lp p) { FENCE(); return *(LDSP v4u*)p; }
DEV v2u lds_rd2(lp p) { FENCE(); return *(LDSP v2u*)p; }
DEV void lds_wr(lp p, u32 v) { *p = v; FENCE(); }
DEV void lds_wr4(lp p, v4u v) { *(LDSP v4u*)p = v; FENCE(); }
DEV void lds_wr2(lp p, v2u v) { *(LDSP v2u*)p = v; FENCE(); }
#define UNDEF(x) asm volatile("" : "=v"(x))

DEV void wait_flag(lp p, int& budget) {
    while (__builtin_amdgcn_readfirstlane(lds_rd(p)) == SENT && --budget > 0) {}
}

DEV bool lnorm(const v4u xp, v4u& yp, int lane) {
    float f[8];
#pragma unroll
    for (int i = 0; i < 4; ++i) { f[2 * i] = blo(xp[i]); f[2 * i + 1] = bhi(xp[i]); }
    const float x0 = __uint_as_float(__builtin_amdgcn_readfirstlane(__float_as_uint(f[0])));
    float s1 = 0.f, s2 = 0.f;
    if (lane < 56) {
        v2f a = {0.f, 0.f}, b = {0.f, 0.f};
#pragma unroll
        for (int i = 0; i < 4; ++i) { v2f e = {f[2 * i] - x0, f[2 * i + 1] - x0}; a += e; b += e * e; }
        s1 = a.x + a.y; s2 = b.x + b.y;
    }
    float t = tsum32(s1, s2);
    t = tsum16(t, t);
    t = rowsum(t);
    const float S1 = rdl(t, 0), S2 = rdl(t, 32);
    const float me = S1 * (1.f / 448.f);
    const float var = fmaxf(S2 * (1.f / 448.f) - me * me, 0.f);
    const u32 mv = cvtpk(x0 + me, var);
    const float mb = blo(mv), vb = bhi(mv);
    const float den = rbf(__builtin_amdgcn_sqrtf(rbf(vb + 1e-5f)));
    const float r = __builtin_amdgcn_rcpf(den);
#pragma unroll
    for (int i = 0; i < 4; ++i) {
        u32 d = cvtpk(f[2 * i] - mb, f[2 * i + 1] - mb);
        yp[i] = cvtpk(blo(d) * r, bhi(d) * r);
    }
    return S1 == S1;
}

DEV int lane_id() { int l; asm volatile("v_mbcnt_lo_u32_b32 %0, -1, 0\n v_mbcnt_hi_u32_b32 %0, -1, %0" : "=v"(l)); __builtin_assume(l >= 0 && l < 64); return l; }
struct Args { const void *xin, *kc, *vc, *wq, *wo, *w1, *b1, *w2, *b2; void *out, *ws; int nlayers; };

DEV void lds_init(lp lds, int tid) {
    v4u s = {SENT, SENT, SENT, SENT};
    *(LDSP v4u*)(lds + 4 * tid) = s;
    if (tid < 2 * L_SZ / 4 - 768) *(LDSP v4u*)(lds + 4 * (tid + 768)) = s;
    __syncthreads();
}

template <bool ISW1>
DEV void kv_role(const Args& A, lp lds, const int w, const int g, const int j, const int wave) {
    const int lane = lane_id(), tid = wave * 64 + lane;
    const int r16 = lane & 15, c4 = lane >> 4;
    const rsrc_t rws = mkr(A.ws, 4u << 20);
    const int c2r = g * 4 + (j - 28);
    const int c2 = c2r < 27 ? c2r : 27;
    int budget = 200000;
    const int u = wave, kh = u >> 2, dqr = u & 3, kq = j >> 3, dq = j & 7;
    const rsrc_t rk = mkr(A.kc, 268435456u), rv = mkr(A.vc, 268435456u);
    v4u K[4][2];
    {
        const u32 kb = (u32)((g * 65536 + 61440 + 128 * j + 64 * kh + r16) * 512 + (64 * dqr + 8 * c4) * 2);
#pragma unroll
        for (int tt = 0; tt < 4; ++tt)
#pragma unroll
            for (int t = 0; t < 2; ++t) K[tt][t] = LD128S(rk, kb, tt * 8192 + t * 64, BULK_AUX);
    }
    FENCE();
    lds_init(lds, tid);
    const u32 vb = (u32)((g * 65536 + 61440 + 1024 * kq + 128 * u + 32 * c4 + 16 * (r16 >> 3)) * 512 + (32 * dq + 4 * (r16 & 7)) * 2);
    v4u VB00, VB01, VB02, VB03, VB10, VB11, VB12, VB13;
    v4u WB00, WB01, WB10, WB11, WB20, WB21, WB30, WB31;
    UNDEF(VB00); UNDEF(VB01); UNDEF(VB02); UNDEF(VB03); UNDEF(VB10); UNDEF(VB11); UNDEF(VB12); UNDEF(VB13);
    UNDEF(WB00); UNDEF(WB01);
    if (!ISW1) { UNDEF(WB10); UNDEF(WB11); UNDEF(WB20); UNDEF(WB21); UNDEF(WB30); UNDEF(WB31); }
    u32 tagl = 0, tagh = 0;
    v4u rwsq;
    { u64 p = (u64)A.ws; rwsq.x = __builtin_amdgcn_readfirstlane((u32)p); rwsq.y = __builtin_amdgcn_readfirstlane((u32)(p >> 32)); rwsq.z = 4u << 20; rwsq.w = 0x00020000u; }
    const int rr = r16 & 7;
    const u32 aoff0 = O_A + g * 2048 + (64 * (rr & 1) + 16 * dqr + 8 * (rr >> 2) + 2 * c4 + ((rr >> 1) & 1)) * 16;
    const u32 soff0 = O_S + g * 16384 + (8 * kq + u) * 512 + (lane & 1) * 256 + (32 * c4 + 16 * ((r16 >> 1) & 1) + 4 * (r16 >> 2)) * 2;
    const u32 moff0 = O_ML + g * 512 + (lane & 1) * 256 + ((lane >> 1) & 15) * 16;
    const u32 poff0 = ISW1 ? O_PB + g * 8192 + ((lane >> 2) & 3) * 2048 + u * 256 + (4 * c4 + 2 * (lane & 1) + ((lane >> 1) & 1)) * 16
                           : O_H + g * 4096 + (256 * u + 32 * (lane & 1) + 8 * c4) * 2;
    const u32 wb = ISW1 ? (u32)((512 * g + 32 * u + 4 * (2 * c4 + (r16 >> 3))) * 896 + (16 * j + 2 * (r16 & 7)) * 2)
                        : (u32)((256 * u + 32 * (r16 >> 3) + 8 * c4) * 896 + (16 * c2 + 2 * (r16 & 7)) * 2);
    const rsrc_t rw = ISW1 ? mkr(A.wo, 4096u * 448u * 2u) : mkr(A.w2, 1792u * 448u * 2u);
#pragma nounroll
    for (int L = 0; L < A.nlayers; ++L) {
        const lp ld = lds + L * L_SZ;
        const bool first = L == 0;
        v2u Vr[16];
#pragma unroll
        for (int i = 0; i < 16; ++i) UNDEF(Vr[i]);
        if (first) {
#pragma unroll
            for (int i = 0; i < N1; ++i) Vr[i] = LD64S(rv, vb, i * 512, BULK_AUX);
        }
        FENCE();
        {
            v4u q;
            do { q = lds_rd4(ld + L_QF); } while (__builtin_amdgcn_readfirstlane(max(max(q.x, q.y), max(q.z, q.w))) == SENT && --budget > 0);
        }
        if (first) {
            v2u tg = lds_rd2(lds + L_TAG);
            tagl = __builtin_amdgcn_readfirstlane(tg.x); tagh = __builtin_amdgcn_readfirstlane(tg.y);
            asm volatile("s_waitcnt vmcnt(%0)" :: "i"(N1) : "memory");
        } else {
            __builtin_amdgcn_s_sleep(D_A);
        }
        TS(1 + 12 * L);
        v4u qa;
        {
            const u32 aoff = aoff0 + L * 16384;
            asm volatile("buffer_load_dwordx4 %0, %1, %2, 0 offen sc1" : "=v"(qa) : "v"(aoff), "s"(rwsq) : "memory");
            if (first) {
#pragma unroll
                for (int i = N1; i < 16; ++i) Vr[i] = LD64S(rv, vb, i * 512, BULK_AUX);
                asm volatile("s_waitcnt vmcnt(%0)" :: "i"(16 - N1) : "memory");
            } else {
                asm volatile("s_waitcnt vmcnt(0)" ::: "memory");
            }
            asm volatile("" : "+v"(qa));
            int tries = 0;
            while (__builtin_expect(!__all(qa.x == tagl && qa.z == tagh), 0)) {
                if ((budget -= 200) < 0) break;
                __builtin_amdgcn_s_sleep(D_R);
                asm volatile("buffer_load_dwordx4 %0, %1, %2, 0 offen sc1\n s_waitcnt vmcnt(0)" : "=v"(qa) : "v"(aoff), "s"(rwsq) : "memory");
                ++tries;
            }
            CNT(28 + L, tries);
        }
        TS(2 + 12 * L);
        {
            v4u A0 = {qa.y, qa.w, dppu<SHL2>(qa.y), dppu<SHL2>(qa.w)};
            v4u A1 = {dppu<SHL4>(qa.y), dppu<SHL4>(qa.w), dppu<SHL6>(qa.y), dppu<SHL6>(qa.w)};
            v4f acc[4];
#pragma unroll
            for (int tt = 0; tt < 4; ++tt) { v4f z = {0.f, 0.f, 0.f, 0.f}; acc[tt] = mfma(A0, K[tt][0], z); }
#pragma unroll
            for (int tt = 0; tt < 4; ++tt) acc[tt] = mfma(A1, K[tt][1], acc[tt]);
            if (lane < 16) {
#pragma unroll
                for (int tt = 0; tt < 4; ++tt) {
                    ld[L_SP + (64 * kh + 16 * tt + lane) * 4 + dqr] = __float_as_uint(acc[tt][0]);
                    ld[L_SP + (128 + 64 * kh + 16 * tt + lane) * 4 + dqr] = __float_as_uint(acc[tt][1]);
                }
            }
            FENCE();
        }
        TS(3 + 12 * L);
        {
            v2u s;
            do { s = lds_rd2(ld + L_SF); } while (__builtin_amdgcn_readfirstlane(max(s.x, s.y)) == SENT && --budget > 0);
        }
        __builtin_amdgcn_s_sleep(D_S);
        TS(4 + 12 * L);
        u32 Wo[8];
#pragma unroll
        for (int i = 0; i < 8; ++i) UNDEF(Wo[i]);
        {
            int tries = 0;
            FENCE();
            v2u sc = LD64S(rws, soff0, L * (8 * 16384), SC1);
            v4u ml = LD128S(rws, moff0, L * (8 * 512), SC1);
            FENCE();
            if (first) {
                if (ISW1) {
#pragma unroll
                    for (int i = 0; i < 8; ++i) Wo[i] = LD32S(rw, wb, (256 * (i >> 2) + (i & 3)) * 896, BULK_AUX);
                }
                FENCE();
#define VPERM(t, ip) { const v2u a = Vr[8 * t + 2 * ip], b = Vr[8 * t + 2 * ip + 1]; \
                       VB##t##0[ip] = plo(a.x, b.x); VB##t##1[ip] = phi(a.x, b.x); VB##t##2[ip] = plo(a.y, b.y); VB##t##3[ip] = phi(a.y, b.y); }
                VPERM(0, 0) VPERM(0, 1) VPERM(0, 2) VPERM(0, 3) VPERM(1, 0) VPERM(1, 1) VPERM(1, 2) VPERM(1, 3)
            }
            FENCE();
            while (__builtin_expect(__any(max(max(sc.x, sc.y), max(ml.y, ml.w)) == SENT), 0)) {
                if ((budget -= 200) < 0) break;
                ++tries;
                __builtin_amdgcn_s_sleep(D_R);
                FENCE();
                sc = LD64S(rws, soff0, L * (8 * 16384), SC1);
                ml = LD128S(rws, moff0, L * (8 * 512), SC1);
                FENCE();
            }
            CNT(30 + L, tries);
            const float CE = 0.0625f * 1.4426950408889634f;
            const float m0 = __uint_as_float(ml.x), l0 = __uint_as_float(ml.y), m1 = __uint_as_float(ml.z), l1 = __uint_as_float(ml.w);
            float M = fmaxf(m0, m1);
            M = fmaxf(M, dppf<XOR2>(M)); M = fmaxf(M, dppf<ROR4>(M)); M = fmaxf(M, dppf<ROR8>(M)); M = tmax16(M);
            float ls = l0 * ex2((m0 - M) * CE) + l1 * ex2((m1 - M) * CE);
            const float e0 = ex2((blo(sc.x) - M) * CE), e1 = ex2((bhi(sc.x) - M) * CE), e2 = ex2((blo(sc.y) - M) * CE), e3 = ex2((bhi(sc.y) - M) * CE);
            ls += dppf<XOR2>(ls); ls += dppf<ROR4>(ls); ls += dppf<ROR8>(ls); ls = tsum16(ls, ls);
            const float rl = __builtin_amdgcn_rcpf(ls);
            const u32 p0 = cvtpk(e0 * rl, e1 * rl), p1 = cvtpk(e2 * rl, e3 * rl);
            v4u A0 = {p0, p1, dppu<SHL4>(p0), dppu<SHL4>(p1)};
            v4u A1 = {dppu<SHL8>(p0), dppu<SHL8>(p1), dppu<SHL12>(p0), dppu<SHL12>(p1)};
            const v4f z = {0.f, 0.f, 0.f, 0.f};
            v4f a0 = mfma(A0, VB00, z), a1 = mfma(A0, VB01, z), a2 = mfma(A0, VB02, z), a3 = mfma(A0, VB03, z);
            a0 = mfma(A1, VB10, a0); a1 = mfma(A1, VB11, a1); a2 = mfma(A1, VB12, a2); a3 = mfma(A1, VB13, a3);
            v4u o0, o1;
            o0[0] = __float_as_uint(a0[0] + dppf<SHL8>(a0[2])); o1[0] = __float_as_uint(a0[1] + dppf<SHL8>(a0[3]));
            o0[1] = __float_as_uint(a1[0] + dppf<SHL8>(a1[2])); o1[1] = __float_as_uint(a1[1] + dppf<SHL8>(a1[3]));
            o0[2] = __float_as_uint(a2[0] + dppf<SHL8>(a2[2])); o1[2] = __float_as_uint(a2[1] + dppf<SHL8>(a2[3]));
            o0[3] = __float_as_uint(a3[0] + dppf<SHL8>(a3[2])); o1[3] = __float_as_uint(a3[1] + dppf<SHL8>(a3[3]));
            if (lane < 8) { *(LDSP v4u*)(ld + L_PP + u * 64 + lane * 8) = o0; *(LDSP v4u*)(ld + L_PP + u * 64 + lane * 8 + 4) = o1; }
            FENCE();
        }
        TS(5 + 12 * L);
        if (ISW1) {
            if (first) {
#pragma unroll
                for (int ip = 0; ip < 4; ++ip) { WB00[ip] = plo(Wo[2 * ip], Wo[2 * ip + 1]); WB01[ip] = phi(Wo[2 * ip], Wo[2 * ip + 1]); }
            }
            FENCE();
            wait_flag(ld + L_PF, budget);
            __builtin_amdgcn_s_sleep(D_PB);
            TS(6 + 12 * L);
            int tries = 0;
            FENCE();
            v4u pb = LD128S(rws, poff0, L * (8 * 8192), SC1);
            FENCE();
            while (__builtin_expect(__any(bad4(pb)), 0)) {
                if ((budget -= 200) < 0) break;
                ++tries;
                __builtin_amdgcn_s_sleep(D_R);
                FENCE();
                pb = LD128S(rws, poff0, L * (8 * 8192), SC1);
                FENCE();
            }
            float o[4];
#pragma unroll
            for (int i = 0; i < 4; ++i) { float v = __uint_as_float(pb[i]); v += dppf<ROR4>(v); v += dppf<ROR8>(v); o[i] = v; }
            const u32 a0 = cvtpk(o[0], o[1]), a1 = cvtpk(o[2], o[3]);
            v4u Aw = {a0, a1, dppu<SHL2>(a0), dppu<SHL2>(a1)};
            v4f z = {0.f, 0.f, 0.f, 0.f};
            v4f d0 = mfma(Aw, WB00, z), d1 = mfma(Aw, WB01, z);
            v2u r = {__float_as_uint(d0[0] + dppf<SHL8>(d0[1])), __float_as_uint(d1[0] + dppf<SHL8>(d1[1]))};
            if (lane < 8) lds_wr2(ld + L_WP + u * 16 + lane * 2, r);
            CNT(26 + L, tries);
            TS(7 + 12 * L);
        } else if (u == 7) {
            if (lane < 8) { v2u z = {0u, 0u}; lds_wr2(ld + L_WP + 7 * 16 + lane * 2, z); }
        } else {
            if (first) {
                u32 Wr[32];
#pragma unroll
                for (int i = 0; i < 32; ++i) Wr[i] = LD32S(rw, wb, (64 * (i >> 3) + (i & 7)) * 896, BULK_AUX);
#define WPERM(t) { _Pragma("unroll") for (int ip = 0; ip < 4; ++ip) { WB##t##0[ip] = plo(Wr[8 * t + 2 * ip], Wr[8 * t + 2 * ip + 1]); WB##t##1[ip] = phi(Wr[8 * t + 2 * ip], Wr[8 * t + 2 * ip + 1]); } }
                WPERM(0) WPERM(1) WPERM(2) WPERM(3)
            }
            FENCE();
            wait_flag(ld + L_XF, budget);
            __builtin_amdgcn_s_sleep(D_H);
            TS(6 + 12 * L);
            int tries = 0;
            for (;;) {
                FENCE();
                v4u hv0, hv1, hv2, hv3;
                UNDEF(hv0); UNDEF(hv1); UNDEF(hv2); UNDEF(hv3);
                if (r16 < 2) {
                    hv0 = LD128S(rws, poff0, L * (8 * 4096), SC1); hv1 = LD128S(rws, poff0, L * (8 * 4096) + 128, SC1);
                    hv2 = LD128S(rws, poff0, L * (8 * 4096) + 256, SC1); hv3 = LD128S(rws, poff0, L * (8 * 4096) + 384, SC1);
                }
                FENCE();
                v4f d0 = {0.f, 0.f, 0.f, 0.f}, d1 = d0;
                d0 = mfma(hv0, WB00, d0); d1 = mfma(hv0, WB01, d1);
                d0 = mfma(hv1, WB10, d0); d1 = mfma(hv1, WB11, d1);
                d0 = mfma(hv2, WB20, d0); d1 = mfma(hv2, WB21, d1);
                d0 = mfma(hv3, WB30, d0); d1 = mfma(hv3, WB31, d1);
                const float r0 = d0[0] + dppf<SHL8>(d0[1]), r1 = d1[0] + dppf<SHL8>(d1[1]);
                const bool bad = lane < 8 && !(r0 == r0);
                if (__builtin_expect(__any(bad), 0) && (budget -= 200) >= 0) { ++tries; __builtin_amdgcn_s_sleep(D_R); continue; }
                v2u r = {__float_as_uint(r0), __float_as_uint(r1)};
                if (lane < 8) lds_wr2(ld + L_WP + u * 16 + lane * 2, r);
                break;
            }
            CNT(26 + L, tries);
            TS(7 + 12 * L);
        }
    }
}

template <bool ISW1>
DEV void aux_role(const Args& A, lp lds, const int w, const int g, const int j, const int wave) {
    const int lane = lane_id(), tid = wave * 64 + lane;
    const int c4 = lane >> 4;
    const rsrc_t rws = mkr(A.ws, 4u << 20);
    const int c2r = g * 4 + (j - 28);
    const int c2 = c2r < 27 ? c2r : 27;
    int budget = 200000;
    const int a = wave - 8;
    const int lam = lane < 56 ? lane : 55;
    const int kq = j >> 3, dq = j & 7;
    const int c1 = g * 28 + j;
    {
        const int ci = a * 64 + lane;
        u32 off = 0xFFFFFFF0u;
        if (ci < 64) off = O_S + ((ci >> 5) * 8 + g) * 16384 + j * 512 + (ci & 31) * 16;
        else if (ci < 96) off = O_PB + (((ci - 64) >> 4) * 8 + g) * 8192 + kq * 2048 + dq * 256 + (ci & 15) * 16;
        else if (ci < 160) { if (ISW1) off = O_PC + ((ci - 96) >> 2) * 16384 + g * 1792 + j * 64 + (ci & 3) * 16; }
        else if (ci < 176) { if (ISW1) off = O_H + (ci - 160) * 4096 + c1 * 16; }
        else if (ci < 192) { if (!ISW1 && c2r < 28) off = O_X2 + ((ci - 176) >> 1) * 1024 + c2 * 32 + (ci & 1) * 16; }
        v4u s = {SENT, SENT, SENT, SENT};
        if (ci < 192) ST128(s, rws, off, SC1);
        else if (ci < 196) { v2u s2 = {SENT, SENT}; ST64(s2, rws, O_ML + (((ci - 192) >> 1) * 8 + g) * 512 + (ci & 1) * 256 + j * 8, SC1); }
    }
    FENCE();
    const v2u ep = LD64(rws, O_EPOCH, SC1);
    v4u xp = LD128(mkr(A.xin, 896), lam * 16, 0);
    v4u QB0, QB1, QB2, QB3;
    {
        const rsrc_t rq = mkr(A.wq, 448u * 4096u * 2u);
        const u32 qb = lane < 56 ? (u32)(8 * lane * 8192 + (512 * g + 16 * j + 4 * a) * 2) : 0xF0000000u;
        v2u Qr[8];
#pragma unroll
        for (int i = 0; i < 8; ++i) Qr[i] = LD64S(rq, qb, i * 8192, BULK_AUX);
        FENCE();
        lds_init(lds, tid);
#pragma unroll
        for (int ip = 0; ip < 4; ++ip) {
            QB0[ip] = plo(Qr[2 * ip].x, Qr[2 * ip + 1].x); QB1[ip] = phi(Qr[2 * ip].x, Qr[2 * ip + 1].x);
            QB2[ip] = plo(Qr[2 * ip].y, Qr[2 * ip + 1].y); QB3[ip] = phi(Qr[2 * ip].y, Qr[2 * ip + 1].y);
        }
    }
    asm volatile("s_waitcnt vmcnt(0)" ::: "memory");
    const u64 tag = (((u64)ep.y << 32) | ep.x) + 1;
    const u32 tagl = (u32)tag, tagh = (u32)(tag >> 32);
    if (a == 0 && lane == 0) { v2u tg = {tagl, tagh}; lds_wr2(lds + L_TAG, tg); }
    v4u W10, W11, W12, W13, W14, W15, W16, W17;
    if (ISW1) { UNDEF(W10); UNDEF(W11); UNDEF(W12); UNDEF(W13); UNDEF(W14); UNDEF(W15); UNDEF(W16); UNDEF(W17); }
    float bias1 = 0.f;
    v4f bias2 = {0.f, 0.f, 0.f, 0.f};
#pragma nounroll
    for (int L = 0; L < A.nlayers; ++L) {
        const lp ld = lds + L * L_SZ;
        const bool first = L == 0;
        v4u yp;
        if (!first) {
            wait_flag(lds + L_HF, budget);
            __builtin_amdgcn_s_sleep(ISW1 ? D_X1 : D_X2);
            TS(1 + 12 * L);
            int tries = 0;
            FENCE();
            xp = LD128(rws, O_X2 + g * 1024 + lam * 16, SC1);
            FENCE();
            while (__builtin_expect(__any(bad4(xp)), 0)) {
                if ((budget -= 200) < 0) break;
                ++tries;
                __builtin_amdgcn_s_sleep(D_R);
                FENCE();
                xp = LD128(rws, O_X2 + g * 1024 + lam * 16, SC1);
                FENCE();
            }
            CNT(28 + L, tries);
            TS(9 + 12 * L);
        }
        lnorm(xp, yp, lane);
        {
#define DOT4(B) dot2(yp[3], B[3], dot2(yp[2], B[2], dot2(yp[1], B[1], dot2(yp[0], B[0], 0.f))))
            const float d0 = DOT4(QB0), d1 = DOT4(QB1), d2 = DOT4(QB2), d3 = DOT4(QB3);
            float z = tsum16(tsum32(d0, d2), tsum32(d1, d3));
            z = rowsum(z);
            auto r1 = __builtin_amdgcn_permlane16_swap(__float_as_uint(z), __float_as_uint(z), false, false);
            const u32 pk = cvtpk(__uint_as_float(r1[0]), __uint_as_float(r1[1]));
            auto r2 = __builtin_amdgcn_permlane32_swap(pk, pk, false, false);
            v4u un = {tagl, r2[0], tagh, r2[1]};
            if (lane == 0) { ST128(un, rws, O_A + L * 16384 + g * 2048 + (4 * j + a) * 16, SC1); lds_wr(ld + L_QF + a, 1u); }
        }
        TS(2 + 12 * L);
        if (a < 2) {
            v4u p0, p1;
            do {
                p0 = lds_rd4(ld + L_SP + (a * 128 + 2 * lane) * 4);
                p1 = *(LDSP v4u*)(ld + L_SP + (a * 128 + 2 * lane) * 4 + 4);
            } while (__any(max(max(max(p0.x, p0.y), max(p0.z, p0.w)), max(max(p1.x, p1.y), max(p1.z, p1.w))) == SENT) && --budget > 0);
            TS(3 + 12 * L);
            const float s0 = (__uint_as_float(p0.x) + __uint_as_float(p0.y)) + (__uint_as_float(p0.z) + __uint_as_float(p0.w));
            const float s1 = (__uint_as_float(p1.x) + __uint_as_float(p1.y)) + (__uint_as_float(p1.z) + __uint_as_float(p1.w));
            const u32 sp = cvtpk(s0, s1);
            v4u sv = {sp, dppu<SHL1>(sp), dppu<SHL2>(sp), dppu<SHL3>(sp)};
            if ((lane & 3) == 0) ST128(sv, rws, O_S + (L * 8 + g) * 16384 + j * 512 + a * 256 + lane * 4, SC1);
            const float a0 = blo(sp), a1 = bhi(sp);
            float m = rowmax(fmaxf(a0, a1));
            m = tmax32(tmax16(m));
            const float CE = 0.0625f * 1.4426950408889634f;
            float l = ex2((a0 - m) * CE) + ex2((a1 - m) * CE);
            l = rowsum(l);
            l = tsum16(l, l); l = tsum32(l, l);
            v2u mlv = {__float_as_uint(m), __float_as_uint(l)};
            if (lane == 0) { ST64(mlv, rws, O_ML + (L * 8 + g) * 512 + a * 256 + j * 8, SC1); lds_wr(ld + L_SF + a, 1u); }
            if (first && a == 0 && j == 0 && lane < 8) { v2u tg = {tagl, tagh}; ST64(tg, rws, O_F + lane * 64 + g * 8, SC1); }
            TS(4 + 12 * L);
        } else if (a == 2) {
            v4u p0, p1;
            const int pw = lane & 3, un = lane >> 2;
            do {
                p0 = lds_rd4(ld + L_PP + pw * 64 + un * 4);
                p1 = *(LDSP v4u*)(ld + L_PP + (pw + 4) * 64 + un * 4);
            } while (__any(max(max(max(p0.x, p0.y), max(p0.z, p0.w)), max(max(p1.x, p1.y), max(p1.z, p1.w))) == SENT) && --budget > 0);
            TS(3 + 12 * L);
            v4u r;
#pragma unroll
            for (int i = 0; i < 4; ++i) { float v = __uint_as_float(p0[i]) + __uint_as_float(p1[i]); v += dppf<XOR1>(v); v += dppf<XOR2>(v); r[i] = __float_as_uint(v); }
            if (pw == 0) ST128(r, rws, O_PB + (L * 8 + g) * 8192 + kq * 2048 + dq * 256 + un * 16, SC1);
            if (lane == 0) lds_wr(ld + L_PF, 1u);
            TS(4 + 12 * L);
        } else if (ISW1) {
            v4u p0;
            const int pw = lane & 7, cq = (lane >> 3) & 3;
            do { p0 = lds_rd4(ld + L_WP + pw * 16 + cq * 4); } while (__any(bad4(p0)) && --budget > 0);
            TS(3 + 12 * L);
            v4u r;
#pragma unroll
            for (int i = 0; i < 4; ++i) { float v = __uint_as_float(p0[i]); v += dppf<XOR1>(v); v += dppf<XOR2>(v); v += dppf<HMIR>(v); r[i] = __float_as_uint(v); }
            if (lane < 32) ST128(r, rws, O_PC + (L * 8 + pw) * 16384 + g * 1792 + j * 64 + cq * 16, SC1);
            if (lane == 0) lds_wr(ld + L_WF, 1u);
            TS(4 + 12 * L);
        }
        if (a == 0) {
            if (first) {
                wait_flag(ld + L_PF, budget);
                if (ISW1) {
                    const rsrc_t r1 = mkr(A.w1, 448u * 1792u * 2u);
                    const u32 wb = lane < 56 ? (u32)(8 * lane * 3584 + c1 * 16) : 0xF0000000u;
                    v4u Wr[8];
#pragma unroll
                    for (int i = 0; i < 8; ++i) Wr[i] = LD128S(r1, wb, i * 3584, BULK_AUX);
                    const int col = (lane & 1) + 2 * c4;
                    bias1 = __uint_as_float((u32)LD16(mkr(A.b1, 3584), (8 * c1 + col) * 2, 0) << 16);
#define W1PERM(X, Y, cp) { _Pragma("unroll") for (int ip = 0; ip < 4; ++ip) { X[ip] = plo(Wr[2 * ip][cp], Wr[2 * ip + 1][cp]); Y[ip] = phi(Wr[2 * ip][cp], Wr[2 * ip + 1][cp]); } }
                    W1PERM(W10, W11, 0) W1PERM(W12, W13, 1) W1PERM(W14, W15, 2) W1PERM(W16, W17, 3)
                } else {
                    v2u bb = LD64(mkr(A.b2, 896), (16 * c2 + 4 * ((lane >> 3) & 3)) * 2, 0);
                    bias2 = v4f{blo(bb.x), bhi(bb.x), blo(bb.y), bhi(bb.y)};
                }
                for (;;) {
                    FENCE();
                    v2u f = LD64(rws, O_F + g * 64 + (lane & 7) * 8, SC1);
                    if (__all(f.x == tagl && f.y == tagh) || (budget -= 200) < 0) break;
                    __builtin_amdgcn_s_sleep(4);
                }
            }
            if (ISW1) { wait_flag(ld + L_WF, budget); __builtin_amdgcn_s_sleep(D_PC); }
            else { wait_flag(ld + L_PF, budget); __builtin_amdgcn_s_sleep(D_PC2); }
            TS(5 + 12 * L);
            v4u x1p, y1;
            int tries = 0;
            const u32 coff = O_PC + (L * 8 + g) * 16384 + lam * 32;
            for (;;) {
                FENCE();
                v4u pc[16];
#pragma unroll
                for (int i = 0; i < 16; ++i) pc[i] = LD128S(rws, coff, (i >> 1) * 1792 + (i & 1) * 16, SC1);
                FENCE();
#pragma unroll
                for (int hh = 0; hh < 2; ++hh) {
                    v4f s[8];
#pragma unroll
                    for (int gg = 0; gg < 8; ++gg) s[gg] = __builtin_bit_cast(v4f, pc[2 * gg + hh]);
                    v4f t = ((s[0] + s[1]) + (s[2] + s[3])) + ((s[4] + s[5]) + (s[6] + s[7]));
                    const u32 a0 = cvtpk(t[0], t[1]), a1 = cvtpk(t[2], t[3]);
                    x1p[2 * hh] = cvtpk(blo(xp[2 * hh]) + blo(a0), bhi(xp[2 * hh]) + bhi(a0));
                    x1p[2 * hh + 1] = cvtpk(blo(xp[2 * hh + 1]) + blo(a1), bhi(xp[2 * hh + 1]) + bhi(a1));
                }
                const bool ok = lnorm(x1p, y1, lane);
                if (__builtin_expect(ok, 1) || (budget -= 200) < 0) break;
                ++tries;
                __builtin_amdgcn_s_sleep(D_R);
            }
            CNT(30 + L, tries);
            TS(6 + 12 * L);
            if (ISW1) {
                v4u y2;
                lnorm(y1, y2, lane);
#define DOT4B(B) dot2(y2[3], B[3], dot2(y2[2], B[2], dot2(y2[1], B[1], dot2(y2[0], B[0], 0.f))))
                const float d0 = DOT4B(W10), d1 = DOT4B(W11), d2 = DOT4B(W12), d3 = DOT4B(W13), d4 = DOT4B(W14), d5 = DOT4B(W15), d6 = DOT4B(W16), d7 = DOT4B(W17);
                const float e0 = tsum16(tsum32(d0, d4), tsum32(d2, d6));
                const float e1 = tsum16(tsum32(d1, d5), tsum32(d3, d7));
                const bool odd = lane & 1;
                float z = (odd ? e1 : e0) + dppf<XOR1>(odd ? e0 : e1);
                z += dppf<XOR2>(z); z += dppf<ROR4>(z); z += dppf<ROR8>(z);
                const float hx = rbf(rbf(z) + bias1);
                const float uu = 0.7978845608028654f * (hx + 0.044715f * hx * hx * hx);
                const float hv = hx * __builtin_amdgcn_rcpf(1.f + ex2(uu * (-2.f * 1.4426950408889634f)));
                const u32 pk = cvtpk(hv, dppf<XOR1>(hv));
                auto r1 = __builtin_amdgcn_permlane16_swap(pk, pk, false, false);
                auto r2 = __builtin_amdgcn_permlane32_swap(r1[0], r1[0], false, false);
                auto r3 = __builtin_amdgcn_permlane32_swap(r1[1], r1[1], false, false);
                v4u hvv = {r2[0], r3[0], r2[1], r3[1]};
                if (lane < 16 && !odd) ST128(hvv, rws, O_H + (L * 8 + (lane >> 1)) * 4096 + c1 * 16, SC1);
                if (lane == 0) lds_wr(ld + L_HF, 1u);
            } else {
                if ((lane >> 1) == c2) lds_wr4(ld + L_X1 + (lane & 1) * 4, x1p);
                if (lane == 0) lds_wr(ld + L_XF, 1u);
                const int pw = lane & 7, cq = (lane >> 3) & 3;
                const v2u xo = lds_rd2(ld + L_X1 + cq * 2);
                v4u p0;
                do { p0 = lds_rd4(ld + L_WP + pw * 16 + cq * 4); } while (__any(bad4(p0)) && --budget > 0);
                TS(7 + 12 * L);
                float r[4];
#pragma unroll
                for (int i = 0; i < 4; ++i) { float v = __uint_as_float(p0[i]); v += dppf<XOR1>(v); v += dppf<XOR2>(v); v += dppf<HMIR>(v); r[i] = v; }
                const u32 t0 = cvtpk(r[0], r[1]), t1 = cvtpk(r[2], r[3]);
                const u32 u0 = cvtpk(blo(t0) + bias2[0], bhi(t0) + bias2[1]), u1 = cvtpk(blo(t1) + bias2[2], bhi(t1) + bias2[3]);
                const u32 o0 = cvtpk(blo(xo.x) + blo(u0), bhi(xo.x) + bhi(u0)), o1 = cvtpk(blo(xo.y) + blo(u1), bhi(xo.y) + bhi(u1));
                v4u ov = {o0, o1, dppu<SHL8>(o0), dppu<SHL8>(o1)};
                const bool st = lane < 32 && (lane & 8) == 0 && c2r < 28;
                if (L + 1 < A.nlayers) {
                    if (st) ST128(ov, rws, O_X2 + pw * 1024 + c2 * 32 + c4 * 16, SC1);
                    if (lane == 0) lds_wr(ld + L_HF, 1u);
                } else {
                    if (st && pw == 0) ST128(ov, mkr(A.out, 896), c2 * 32 + c4 * 16, 0);
                    if (c2r == 0 && lane == 0) { v2u tg = {tagl, tagh}; ST64(tg, rws, O_EPOCH, SC1); }
                }
            }
            TS(8 + 12 * L);
        }
    }
}

__global__ void __launch_bounds__(768, 3)
fused_decode(const void* __restrict__ xin, const void* __restrict__ kc, const void* __restrict__ vc, const void* __restrict__ wq,
             const void* __restrict__ wo, const void* __restrict__ w1, const void* __restrict__ b1, const void* __restrict__ w2,
             const void* __restrict__ b2, void* __restrict__ out, void* __restrict__ ws, int nlayers) {
    __shared__ __attribute__((aligned(16))) u32 lds_[2 * L_SZ];
    const lp lds = (lp)lds_;
    const Args A = {xin, kc, vc, wq, wo, w1, b1, w2, b2, out, ws, nlayers};
    const int w = blockIdx.x, g = w & 7, j = w >> 3;
    const int wave = __builtin_amdgcn_readfirstlane(threadIdx.x >> 6);
    if (wave < 8) {
        if (j < 28) kv_role<true>(A, lds, w, g, j, wave); else kv_role<false>(A, lds, w, g, j, wave);
    } else {
        if (j < 28) aux_role<true>(A, lds, w, g, j, wave); else aux_role<false>(A, lds, w, g, j, wave);
    }
}

extern "C" void kernel_launch(void* const* d_in, const int* in_sizes, int n_in, void* d_out, int out_size, void* d_ws, size_t ws_size, hipStream_t stream) {
    fused_decode<<<256, 768, 0, stream>>>(d_in[0], d_in[1], d_in[2], d_in[3], d_in[4], d_in[6], d_in[7], d_in[8], d_in[9], d_out, d_ws, 2);
}
```

```cpp
#define SMW 1
#define WNT 1
#define QL 5
#define ECK 1
#define N0 3
#define T_S 700
#define T_W 770
#define T_PB 960
#define G_A0 90
#define G_S0 120
#define G_PB0 50
#include <hip/hip_runtime.h>
#include <stdint.h>

typedef unsigned u32;
typedef unsigned long long u64;
typedef u32 v4u __attribute__((ext_vector_type(4)));
typedef u32 v2u __attribute__((ext_vector_type(2)));
typedef float v4f __attribute__((ext_vector_type(4)));
typedef float v2f __attribute__((ext_vector_type(2)));
typedef short v4s __attribute__((ext_vector_type(4)));
typedef __bf16 v8bf __attribute__((ext_vector_type(8)));
typedef __bf16 v2bf __attribute__((ext_vector_type(2)));

#define DEV __device__ __forceinline__
constexpr u32 SENT = 0xFFFFFFFFu, OOB = 0xF0000000u;

constexpr u32 O_EPOCH = 0, O_F = 1024, O_BC = 4096, O_PCT = 12288, O_XT = 16384, O_X2 = 20480, O_H = 65536, O_ML = 98304,
              O_A0 = 163840, O_A1 = 688128, O_S = 950272, O_PB = 1212416, O_PC = 1474560, O_END = 1736704, O_DBG = 2u << 20,
              O_S0 = 3u << 20, O_ML0 = O_S0 + 262144;

#define KREL F_Q
#define N1 0
#define QX 0
#define VX 1
#define W2PF 1
#define G_W 40
#define G_AX 0
#define G_PC0 100
#define C_H0 195
#define C_X0 320
#define D_X 18
#define D_XP 55
#define C_A1 105
#define C_S1 235
#define C_PB1 365
#define C_PC1 480
#define C_H1 645
#define P_A1 30
#define P_S1 40
#define P_PB1 35
#define P_H 25
#define D_R 1

constexpr u32 L_K = 0, L_V = 65536, L_Q = 131072, L_SP = 145408, L_PP = 153600, L_WP = 157696, L_FL = 158720, L_DBG = 159232, L_PFS = 160768, L_END = 161024;
constexpr int F_Q = 0, F_S = 4, F_P = 6, F_WL = 8, F_OK = 16, F_TB = 17, F_TX = 18, F_TH = 20, F_TAG = 24, F_K = 32, F_W = 40, F_G = 48, F_C = 56, F_A = 64, F_W8 = 72, F_V = 80;

#define FENCE() asm volatile("" ::: "memory")
#define PIN(x) asm volatile("" : "+v"(x))
#define UNDEF(x) asm volatile("" : "=v"(x))
#define VMW(n) asm volatile("s_waitcnt vmcnt(%0)" :: "i"(n) : "memory")

template <int C> DEV u32 dppu(u32 v) { return __builtin_amdgcn_update_dpp(0u, v, C, 0xF, 0xF, true); }
template <int C> DEV float dppf(float v) { return __uint_as_float(dppu<C>(__float_as_uint(v))); }
constexpr int XOR1 = 0xB1, XOR2 = 0x4E, HMIR = 0x141, MIR = 0x140, ROR4 = 0x124, ROR8 = 0x128;
constexpr int SHL2 = 0x102, SHL4 = 0x104, SHL8 = 0x108, SHL12 = 0x10C, QB0 = 0x00, QB1 = 0x55, QB2 = 0xAA, QB3 = 0xFF;

DEV u32 cvtpk(float a, float b) { v2f f = {a, b}; v2bf h = __builtin_convertvector(f, v2bf); return __builtin_bit_cast(u32, h); }
DEV float blo(u32 p) { return __uint_as_float(p << 16); }
DEV float bhi(u32 p) { return __uint_as_float(p & 0xFFFF0000u); }
DEV float rbf(float a) { return blo(cvtpk(a, a)); }
DEV float asf(u32 v) { return __uint_as_float(v); }
DEV u32 asu(float v) { return __float_as_uint(v); }
DEV v4f mfma(v4u a, v4u b) { v4f z = {0.f, 0.f, 0.f, 0.f}; return __builtin_amdgcn_mfma_f32_16x16x32_bf16(__builtin_bit_cast(v8bf, a), __builtin_bit_cast(v8bf, b), z, 0, 0, 0); }
DEV float tsum32(float a, float b) { auto r = __builtin_amdgcn_permlane32_swap(asu(a), asu(b), false, false); return asf(r[0]) + asf(r[1]); }
DEV float tsum16(float a, float b) { auto r = __builtin_amdgcn_permlane16_swap(asu(a), asu(b), false, false); return asf(r[0]) + asf(r[1]); }
DEV float tmax16(float a) { auto r = __builtin_amdgcn_permlane16_swap(asu(a), asu(a), false, false); return fmaxf(asf(r[0]), asf(r[1])); }
DEV float tmax32(float a) { auto r = __builtin_amdgcn_permlane32_swap(asu(a), asu(a), false, false); return fmaxf(asf(r[0]), asf(r[1])); }
DEV float rowsum(float v) { v += dppf<XOR1>(v); v += dppf<XOR2>(v); v += dppf<HMIR>(v); v += dppf<MIR>(v); return v; }
DEV float rowmax(float v) { v = fmaxf(v, dppf<XOR1>(v)); v = fmaxf(v, dppf<XOR2>(v)); v = fmaxf(v, dppf<HMIR>(v)); v = fmaxf(v, dppf<MIR>(v)); return v; }
DEV float rdl(float v, int l) { return asf(__builtin_amdgcn_readlane(asu(v), l)); }
DEV u32 max4(v4u v) { return max(max(v.x, v.y), max(v.z, v.w)); }
DEV float ex2(float x) { return __builtin_amdgcn_exp2f(x); }
DEV u32 rfl(u32 v) { return __builtin_amdgcn_readfirstlane(v); }
DEV int imax_all(int v) {
    v = max(v, (int)dppu<XOR1>((u32)v)); v = max(v, (int)dppu<XOR2>((u32)v)); v = max(v, (int)dppu<HMIR>((u32)v)); v = max(v, (int)dppu<MIR>((u32)v));
    auto r = __builtin_amdgcn_permlane16_swap((u32)v, (u32)v, false, false); v = max((int)r[0], (int)r[1]);
    auto q = __builtin_amdgcn_permlane32_swap((u32)v, (u32)v, false, false); v = max((int)q[0], (int)q[1]);
    return (int)rfl((u32)v);
}

DEV v4u mkq(const void* p, u32 bytes) { u64 q = (u64)p; v4u r; r.x = rfl((u32)q); r.y = rfl((u32)(q >> 32)); r.z = rfl(bytes); r.w = 0x00020000u; return r; }
template <int OFF> DEV void g128(v4u& d, u32 vo, v4u r, u32 so) { asm volatile("buffer_load_dwordx4 %0, %1, %2, %3 offen offset:%4 sc1" : "=v"(d) : "v"(vo), "s"(r), "s"(so), "i"(OFF) : "memory"); }
template <int OFF> DEV void g64(v2u& d, u32 vo, v4u r, u32 so) { asm volatile("buffer_load_dwordx2 %0, %1, %2, %3 offen offset:%4 sc1" : "=v"(d) : "v"(vo), "s"(r), "s"(so), "i"(OFF) : "memory"); }
template <int OFF> DEV void g32(u32& d, u32 vo, v4u r, u32 so) { asm volatile("buffer_load_dword %0, %1, %2, %3 offen offset:%4 sc1" : "=v"(d) : "v"(vo), "s"(r), "s"(so), "i"(OFF) : "memory"); }
template <int OFF> DEV void s128(v4u d, u32 vo, v4u r, u32 so) { asm volatile("buffer_store_dwordx4 %0, %1, %2, %3 offen offset:%4 sc1\n\ts_nop 1" :: "v"(d), "v"(vo), "s"(r), "s"(so), "i"(OFF) : "memory"); }
template <int OFF> DEV void s64(v2u d, u32 vo, v4u r, u32 so) { asm volatile("buffer_store_dwordx2 %0, %1, %2, %3 offen offset:%4 sc1\n\ts_nop 1" :: "v"(d), "v"(vo), "s"(r), "s"(so), "i"(OFF) : "memory"); }
template <int OFF> DEV void s32(u32 d, u32 vo, v4u r, u32 so) { asm volatile("buffer_store_dword %0, %1, %2, %3 offen offset:%4 sc1" :: "v"(d), "v"(vo), "s"(r), "s"(so), "i"(OFF) : "memory"); }
DEV void dma(v4u r, u32 vo, u32 so, u32 ldsb) {
    asm volatile("s_mov_b32 m0, %3\n\ts_nop 0\n\tbuffer_load_dwordx4 %0, %1, %2 offen nt lds" :: "v"(vo), "s"(r), "s"(so), "s"(ldsb) : "memory");
}
DEV void dmak(v4u r, u32 vo, u32 so, u32 ldsb) {
    asm volatile("s_mov_b32 m0, %3\n\ts_nop 0\n\tbuffer_load_dwordx4 %0, %1, %2 offen nt lds" :: "v"(vo), "s"(r), "s"(so), "s"(ldsb) : "memory");
}

#define LDSP __attribute__((address_space(3)))
typedef LDSP char* lp;
DEV u32 lds_rd(lp p) { FENCE(); return *(LDSP u32*)p; }
DEV v2u lds_rd2(lp p) { FENCE(); return *(LDSP v2u*)p; }
DEV v4u lds_rd4(lp p) { FENCE(); return *(LDSP v4u*)p; }
DEV void lds_wr(lp p, u32 v) { *(LDSP u32*)p = v; FENCE(); }
DEV void lds_wr2(lp p, v2u v) { *(LDSP v2u*)p = v; FENCE(); }
DEV void lds_wr4(lp p, v4u v) { *(LDSP v4u*)p = v; FENCE(); }
DEV v2u trd(lp p) { return __builtin_bit_cast(v2u, __builtin_amdgcn_ds_read_tr16_b64_v4i16((LDSP v4s*)p)); }
DEV v4u trd2(lp p0, lp p1) { const v2u a = trd(p0), b = trd(p1); return v4u{a.x, a.y, b.x, b.y}; }

DEV u32 now32() { return (u32)__builtin_amdgcn_s_memrealtime(); }
DEV void spin_until(u32 t) {
    int n = 3000, d;
#pragma nounroll
    while ((d = (int)(t - now32())) > 0 && --n > 0) { if (d > 12) __builtin_amdgcn_s_sleep(2); }
}
DEV void touch(const void* p) {
    const u64 q = (u64)p, r = (u64)rfl((u32)q) | ((u64)rfl((u32)(q >> 32)) << 32);
    u32 d; asm volatile("s_load_dword %0, %1, 0x0\n\ts_waitcnt lgkmcnt(0)" : "=s"(d) : "s"(r) : "memory");
}
DEV u32 fixs(u32 t) { return t == SENT ? SENT - 1 : t; }
DEV void wait_eq(lp p, u32 v, int& budget) { while (rfl(lds_rd(p)) != v && --budget > 0) {} }
DEV void wait_eq2(lp p, u32 v, int& budget) { for (;;) { v2u q = lds_rd2(p); if ((rfl(q.x) == v && rfl(q.y) == v) || --budget <= 0) break; } }
DEV u32 wait_set(lp p, int& budget) { u32 v; while ((v = rfl(lds_rd(p))) == SENT && --budget > 0) __builtin_amdgcn_s_sleep(1); return v; }
DEV void wait_z8(lp p, int& budget) { for (;;) { v4u q = lds_rd4(p), r = lds_rd4(p + 16); if (rfl(max(max4(q), max4(r))) == 0 || --budget <= 0) break; } }
DEV void wait_ge(lp p, int v, int& budget) { while ((int)rfl(lds_rd(p)) < v && --budget > 0) {} }
DEV int opq(int v) { PIN(v); return v; }
#define PRIO(n) __builtin_amdgcn_s_setprio(n)
#define SBAR() __builtin_amdgcn_sched_barrier(0)

#define TS(id) do {} while (0)
#define CNT(id, v) do {} while (0)
#define DUMP() do {} while (0)

DEV void lnorm(const v4u xp, v4u& yp, bool valid) {
    float f[8];
#pragma unroll
    for (int i = 0; i < 4; ++i) { f[2 * i] = blo(xp[i]); f[2 * i + 1] = bhi(xp[i]); }
    const float x0 = asf(rfl(asu(f[0])));
    v2f a = {0.f, 0.f}, b = {0.f, 0.f};
#pragma unroll
    for (int i = 0; i < 4; ++i) { v2f e = {f[2 * i] - x0, f[2 * i + 1] - x0}; a += e; b += e * e; }
    const float s1 = valid ? a.x + a.y : 0.f, s2 = valid ? b.x + b.y : 0.f;
    float t = tsum32(s1, s2);
    t = tsum16(t, t);
    t = rowsum(t);
    const float S1 = rdl(t, 0), S2 = rdl(t, 32);
    const float me = S1 * (1.f / 448.f);
    const float var = fmaxf(S2 * (1.f / 448.f) - me * me, 0.f);
    const u32 mv = cvtpk(x0 + me, var);
    const float mb = blo(mv), vb = bhi(mv);
    const float den = rbf(__builtin_amdgcn_sqrtf(rbf(vb + 1e-5f)));
    const float r = __builtin_amdgcn_rcpf(den);
#pragma unroll
    for (int i = 0; i < 4; ++i) {
        const u32 d = cvtpk(f[2 * i] - mb, f[2 * i + 1] - mb);
        yp[i] = cvtpk(blo(d) * r, bhi(d) * r);
    }
}

DEV int lane_id() { int l; asm volatile("v_mbcnt_lo_u32_b32 %0, -1, 0\n v_mbcnt_hi_u32_b32 %0, -1, %0" : "=v"(l)); __builtin_assume(l >= 0 && l < 64); return l; }
struct Args { const void *xin, *kc, *vc, *wq, *wo, *w1, *b1, *w2, *b2; void *out, *ws; int nlayers; };

DEV void lds_init(lp lds, int tid) {
    const v4u s = {SENT, SENT, SENT, SENT};
    *(LDSP v4u*)(lds + L_SP + tid * 16) = s;
    if (tid < (int)(L_DBG - L_SP) / 16 - 768) *(LDSP v4u*)(lds + L_SP + (768 + tid) * 16) = s;
    FENCE();
}
DEV void wg_barrier() { asm volatile("s_waitcnt lgkmcnt(0)\n\ts_barrier" ::: "memory"); }

constexpr float CE = 0.0625f * 1.4426950408889634f;

DEV void kv_role(const Args& A, lp lds, const int w, const int wave) {
    const int lane = lane_id();
    const int x = w & 7, s = w >> 3, u = wave - 4;
    const int r16 = lane & 15, c4 = lane >> 4;
    const v4u rws = mkq(A.ws, 4u << 20);
    const int role = x < 4 ? (s < 28 ? 1 : 0) : (x < 6 ? (s < 28 ? 2 : 0) : (s < 14 ? 3 : 0));
    int budget = 400000;
    const int kh = u & 1, dqr = u >> 1, kq = s >> 2, dq = s & 3, gV = x ^ VX, gQ = x ^ QX;
    TS(0);
    const u32 t00 = now32();
    PRIO(1);
    const u32 kbase = L_K + u * 8192, vbase = L_V + u * 8192;
    const int P = (x + 1) & 3, gO = 2 * P + (s >= 14);
    u32 tga;
    {
        const int co = s >= 14 ? s - 14 : s, c1 = (x - 4) * 28 + s, c2 = (x - 6) * 14 + s;
        const int Lc = lane >> 5, hc = (lane >> 4) & 1;
        u32 off = OOB;
        if (u == 0) off = O_A1 + (gQ * 4 + c4) * 8192 + (s >> 3) * 2048 + (s & 7) * 256 + r16 * 16;
        else if (u == 1) { if (lane < 32) off = O_S + (8 + x) * 16384 + hc * 8192 + s * 256 + r16 * 16; }
        else if (u == 2) off = O_PB + (Lc * 8 + gV) * 16384 + kq * 2048 + hc * 1024 + dq * 256 + r16 * 16;
        else if (u == 3) { if (lane < 8) off = O_ML + ((8 + x) * 8 + lane) * 512 + s * 16; }
        else if (u < 6) {
            if (role == 1) off = O_PC + ((u - 4) * 8 + (lane >> 3)) * 16384 + gO * 2048 + (lane & 1) * 1024 + co * 64 + ((lane & 7) >> 1) * 16;
            else if (role == 2) { if (u == 4 && lane < 32) off = O_H + (lane >> 2) * 4096 + c1 * 64 + (lane & 3) * 16; }
            else if (role == 3) { if (u == 4) off = O_X2 + (lane >> 1) * 1024 + c2 * 32 + (lane & 1) * 16; }
        } else if (u == 6) {
            if (role == 1) { if (lane < 2) off = O_PCT + lane * 512 + (28 * x + s) * 4; }
            else if (role == 3) {
                if (lane < 32) off = O_XT + lane * 128 + c2 * 4;
                else if (s == 0) { const int i = (lane - 32) >> 3; off = O_BC + ((x == 6 ? (i < 3 ? i : 7) : 3 + i) * 8 + (lane & 7)) * 128; }
            }
        }
        if (u < 6) s128<0>(v4u{SENT, SENT, SENT, SENT}, off, rws, 0); else s32<0>(SENT, off, rws, 0);
    }
    {
        const v4u rk = mkq(A.kc, 268435456u);
        const u32 ko = (u32)((x * 65536 + 61440 + 128 * s + 64 * kh + (lane >> 3)) * 512 + dqr * 128 + (((lane & 7) ^ c4) * 16));
        const u32 ko1 = ko ^ 64;
        lds_init(lds, wave * 64 + lane);
        wg_barrier();
#pragma unroll
        for (int i = 0; i < N0; ++i) dma(rk, (i & 1) ? ko1 : ko, i * 4096, kbase + i * 1024);
        VMW(N0);
        if (lane == 0) lds_wr(lds + L_FL + (F_C + u) * 4, 0u);
        TS(24);
        for (;;) { v4u q = lds_rd4(lds + L_FL + KREL * 4); if (rfl(max4(q)) == 0 || --budget <= 0) break; }
        tga = now32() + G_A0 + (x == 1 ? G_AX : 0);
#pragma unroll
        for (int i = N0; i < 8; ++i) dma(rk, (i & 1) ? ko1 : ko, i * 4096, kbase + i * 1024);
    }
    const u32 ablk = (u32)(16 * (r16 >> 2) + 8 * (r16 & 1) + u), aqd = (u32)(2 * c4 + ((r16 >> 1) & 1));
    const u32 a0off = O_A0 + (x * 4 + (s & 3)) * 16384 + ablk * 256 + aqd * 16;
    const u32 a1off = O_A1 + (x * 4 + (s & 3)) * 8192 + ablk * 128 + aqd * 16;
    const u32 soff = O_S + gV * 16384 + (r16 & 1) * 8192 + (512 * kq + 64 * u + 32 * ((r16 >> 1) & 1) + 16 * ((r16 >> 2) & 1) + 4 * c4 + 2 * (r16 >> 3)) * 2;
    const u32 moff = O_ML + (gV * 8 + u) * 512 + lane * 8;
    u32 goff = 0;
    if (role == 1) goff = O_PB + gO * 16384 + (2 * (r16 >> 2)) * 2048 + u * 256 + (32 * (r16 & 1) + 16 * ((r16 >> 1) & 1) + 4 * c4) * 4;
    else if (role == 3) goff = O_H + (s & 3) * 4096 + (224 * u + 32 * (r16 < 7 ? r16 : 6) + 8 * c4) * 2;
    const u32 foff = O_F + (s >> 1) * 128 + (lane & 7) * 8;
    const int tq = (lane >> 2) & 3, tp = lane & 3;
    v4u K[4][2], V[4][2];
#pragma unroll
    for (int i = 0; i < 4; ++i) { UNDEF(K[i][0]); UNDEF(K[i][1]); UNDEF(V[i][0]); UNDEF(V[i][1]); }
    u32 Tq = 0, tagl = 0, tagh = 0;
#pragma nounroll
    for (int L = 0; L < A.nlayers; ++L) {
        const bool first = L == 0;
        float q0, q1, q2, q3;
        if (first) {
            for (;;) { v4u q = lds_rd4(lds + L_FL + F_Q * 4); if (rfl(max4(q)) == 0 || --budget <= 0) break; }
            const u32 tg0 = tga;
            const v2u tg = lds_rd2(lds + L_FL + F_TAG * 4);
            tagl = rfl(tg.x); tagh = rfl(tg.y);
            spin_until(tg0);
            TS(1);
            v4u b0, b1;
            g128<0>(b0, a0off, rws, 0); g128<128>(b1, a0off, rws, 0);
            if (lane == 0) lds_wr(lds + L_FL + (F_A + u) * 4, 0u);
            VMW(N1 + 2);
            TS(25);
            if (lane == 0) lds_wr(lds + L_FL + (F_K + u) * 4, 0u);
            {
                const u32 ko = r16 * 256 + (((4 * kh + c4) ^ (r16 & 7)) * 16);
                const lp k0 = lds + kbase + ko, k1 = lds + (kbase ^ 8192) + ko;
#pragma unroll
                for (int tt = 0; tt < 4; ++tt) K[tt][0] = *(LDSP v4u*)(k0 + (tt >> 1) * 4096 + (tt & 1) * 128);
                wait_eq(lds + L_FL + (F_K + (u ^ 1)) * 4, 0, budget);
                FENCE();
#pragma unroll
                for (int tt = 0; tt < 4; ++tt) K[tt][1] = *(LDSP v4u*)(k1 + (tt >> 1) * 4096 + (tt & 1) * 128);
            }
            VMW(0);
            PIN(b0); PIN(b1);
            int tries = 0;
            while (__builtin_expect(!__all(((b0.z ^ tagl) | (b1.z ^ tagl) | (b0.w ^ tagh) | (b1.w ^ tagh)) == 0), 0)) {
                if ((budget -= 200) < 0) break;
                __builtin_amdgcn_s_sleep(D_R);
                g128<0>(b0, a0off, rws, 0); g128<128>(b1, a0off, rws, 0);
                VMW(0);
                PIN(b0); PIN(b1);
                ++tries;
            }
            CNT(26, tries);
            q0 = asf(b0.x); q1 = asf(b0.y); q2 = asf(b1.x); q3 = asf(b1.y);
        } else {
            Tq = wait_set(lds + L_FL + F_TX * 4, budget);
            spin_until(Tq + C_A1);
            TS(13);
            v4u b0;
            int tries = 0;
            for (;;) {
                g128<0>(b0, a1off, rws, 0);
                VMW(0);
                PIN(b0);
                if (__builtin_expect(!__any(max4(b0) == SENT), 1) || (budget -= 200) < 0) break;
                __builtin_amdgcn_s_sleep(D_R);
                ++tries;
            }
            CNT(27, tries);
            q0 = asf(b0.x); q1 = asf(b0.y); q2 = asf(b0.z); q3 = asf(b0.w);
        }
        TS(2 + 12 * L);
        {
            q0 += dppf<ROR4>(q0); q1 += dppf<ROR4>(q1); q2 += dppf<ROR4>(q2); q3 += dppf<ROR4>(q3);
            q0 += dppf<ROR8>(q0); q1 += dppf<ROR8>(q1); q2 += dppf<ROR8>(q2); q3 += dppf<ROR8>(q3);
            const u32 a0 = cvtpk(q0, q1), a1 = cvtpk(q2, q3);
            const v4u Aq = {a0, a1, dppu<SHL2>(a0), dppu<SHL2>(a1)};
            v4f d0[4], d1[4];
#pragma unroll
            for (int tt = 0; tt < 4; ++tt) d0[tt] = mfma(Aq, K[tt][0]);
#pragma unroll
            for (int tt = 0; tt < 4; ++tt) d1[tt] = mfma(Aq, K[tt][1]);
            if (lane < 16) {
                const lp p0 = lds + L_SP + u * 512 + kh * 256 + lane * 16, p1 = lds + L_SP + u * 512 + (kh ^ 1) * 256 + lane * 16;
                *(LDSP v4u*)p0 = v4u{asu(d0[0][0]), asu(d0[1][0]), asu(d0[2][0]), asu(d0[3][0])};
                *(LDSP v4u*)(p0 + 4096) = v4u{asu(d0[0][1]), asu(d0[1][1]), asu(d0[2][1]), asu(d0[3][1])};
                *(LDSP v4u*)p1 = v4u{asu(d1[0][0]), asu(d1[1][0]), asu(d1[2][0]), asu(d1[3][0])};
                *(LDSP v4u*)(p1 + 4096) = v4u{asu(d1[0][1]), asu(d1[1][1]), asu(d1[2][1]), asu(d1[3][1])};
            }
            FENCE();
        }
        TS(3 + 12 * L);
        u32 sc; v2u ml, fc = {0, 0};
        {
            const int z = 2 * (c4 & 1) + (tq >> 1);
            const lp vb = lds + vbase + (4 * c4 + tq) * 128 + tp * 8;
            const lp v0 = vb + (z * 32), v1 = vb + ((1 ^ z) * 32), v2 = vb + ((2 ^ z) * 32), v3 = vb + ((3 ^ z) * 32);
#define VRD(t, e) { const v2u a = trd(v0 + t * 4096 + e * 2048), b = trd(v1 + t * 4096 + e * 2048), c = trd(v2 + t * 4096 + e * 2048), d = trd(v3 + t * 4096 + e * 2048); \
                    V[0][t][2 * e] = a.x; V[0][t][2 * e + 1] = a.y; V[1][t][2 * e] = b.x; V[1][t][2 * e + 1] = b.y; V[2][t][2 * e] = c.x; V[2][t][2 * e + 1] = c.y; V[3][t][2 * e] = d.x; V[3][t][2 * e + 1] = d.y; }
            if (first) {
                wait_eq2(lds + L_FL + F_S * 4, 0, budget);
                u32 tg0 = now32() + G_S0;
                if ((int)(tg0 - (t00 + T_S)) > 0) tg0 = t00 + T_S;
                spin_until(tg0);
                TS(4);
                v4u s4, m4;
                int ln = lane; PIN(ln);
                const u32 so0 = O_S0 + gV * 32768 + (ln & 1) * 16384 + (128 * kq + 16 * u + 8 * ((ln >> 1) & 1) + 4 * ((ln >> 2) & 1) + (ln >> 4)) * 16;
                const u32 mo0 = O_ML0 + (gV * 8 + u) * 1024 + ln * 16;
                g128<0>(s4, so0, rws, 0); g128<0>(m4, mo0, rws, 0); g64<0>(fc, foff, rws, 0);
                const lp fv = lds + L_FL + (F_V + (u >> 2)) * 4;
                if (N1 < 2) wait_ge(fv, 2, budget);
                FENCE(); VRD(0, 0) FENCE();
                if (N1 < 4) wait_ge(fv, 4, budget);
                FENCE(); VRD(0, 1) FENCE();
                {
                    u32 t_; int st_, n_ = 8; u64 c_;
                    const u64 tg_ = (u64)tagl | ((u64)tagh << 32);
                    asm volatile("1:\n\tds_read_b32 %3, %7\n\ts_waitcnt lgkmcnt(0)\n\tv_readfirstlane_b32 %4, %3\n\ts_cmp_gt_i32 %4, 7\n\ts_cbranch_scc1 2f\n\t"
                                 "s_sub_u32 %5, %5, 1\n\ts_cbranch_scc1 2f\n\ts_waitcnt vmcnt(0)\n\t"
                                 "v_cmp_ne_u64 vcc, %8, v[70:71]\n\tv_cmp_ne_u64 %6, %8, v[66:67]\n\ts_or_b64 vcc, vcc, %6\n\ts_cbranch_scc0 2f\n\t"
                                 "buffer_load_dwordx4 %0, %9, %12, 0 offen sc1\n\tbuffer_load_dwordx4 %1, %10, %12, 0 offen sc1\n\tbuffer_load_dwordx2 %2, %11, %12, 0 offen sc1\n\t"
                                 "s_branch 1b\n2:"
                                 : "+{v[68:71]}"(s4), "+{v[64:67]}"(m4), "+v"(fc), "=&v"(t_), "=&s"(st_), "+s"(n_), "=&s"(c_)
                                 : "v"((u32)(size_t)fv), "s"(tg_), "v"(so0), "v"(mo0), "v"(foff), "s"(rws) : "memory", "vcc", "scc");
                }
                wait_ge(fv, 6, budget); FENCE(); VRD(1, 0) FENCE();
                wait_ge(fv, 8, budget); FENCE(); VRD(1, 1) FENCE();
                lds_wr4(lds + vbase + lane * 16, v4u{SENT, SENT, SENT, SENT}); lds_wr4(lds + vbase + 1024 + lane * 16, v4u{SENT, SENT, SENT, SENT});
                TS(9);
                VMW(0);
                PIN(fc); PIN(s4); PIN(m4);
                int tries = 0;
                while (__builtin_expect(!__all(((s4.z ^ tagl) | (m4.z ^ tagl) | (s4.w ^ tagh) | (m4.w ^ tagh)) == 0), 0)) {
                    if ((budget -= 200) < 0) break;
                    __builtin_amdgcn_s_sleep(D_R);
                    g128<0>(s4, so0, rws, 0); g128<0>(m4, mo0, rws, 0); g64<0>(fc, foff, rws, 0);
                    VMW(0);
                    PIN(s4); PIN(m4); PIN(fc);
                    ++tries;
                }
                if (lane == 0) lds_wr(lds + L_FL + (F_G + u) * 4, 0u);
                CNT(28, tries);
                sc = (ln & 8) ? s4.y : s4.x; ml.x = m4.x; ml.y = m4.y;
            } else {
                spin_until(Tq + C_S1);
                TS(16);
                int tries = 0;
                for (;;) {
                    g32<0>(sc, soff, rws, 8 * 16384); g64<0>(ml, moff, rws, 64 * 512);
                    VMW(0);
                    PIN(sc); PIN(ml);
                    if (__builtin_expect(!__any(max(sc, max(ml.x, ml.y)) == SENT), 1) || (budget -= 200) < 0) break;
                    __builtin_amdgcn_s_sleep(D_R);
                    ++tries;
                }
                CNT(29, tries);
            }
        }
        TS(5 + 12 * L);
        {
            const float m0 = asf(ml.x);
            float M = fmaxf(m0, dppf<XOR2>(m0)); M = fmaxf(M, dppf<ROR4>(M)); M = fmaxf(M, dppf<ROR8>(M)); M = tmax32(tmax16(M));
            float ls = asf(ml.y) * ex2((m0 - M) * CE);
            const float e0 = ex2((blo(sc) - M) * CE), e1 = ex2((bhi(sc) - M) * CE);
            ls += dppf<XOR2>(ls); ls += dppf<ROR4>(ls); ls += dppf<ROR8>(ls); ls = tsum16(ls, ls); ls = tsum32(ls, ls);
            const float rl = __builtin_amdgcn_rcpf(ls);
            const u32 p = cvtpk(e0 * rl, e1 * rl);
            const v4u Ap = {p, dppu<SHL8>(p), dppu<SHL4>(p), dppu<SHL12>(p)};
            v4f d0[4], d1[4];
#pragma unroll
            for (int d = 0; d < 4; ++d) { d0[d] = mfma(Ap, V[d][0]); d1[d] = mfma(Ap, V[d][1]); }
            v4u o0, o1;
#pragma unroll
            for (int d = 0; d < 4; ++d) { o0[d] = asu(d0[d][0] + d1[d][2]); o1[d] = asu(d0[d][1] + d1[d][3]); }
            if (lane < 16) { *(LDSP v4u*)(lds + L_PP + u * 256 + lane * 16) = o0; *(LDSP v4u*)(lds + L_PP + 2048 + u * 256 + lane * 16) = o1; }
            FENCE();
        }
        TS(6 + 12 * L);
        if (first && (role == 1 || u == 0)) {
            while (__builtin_expect(!__all(fc.x == tagl && fc.y == tagh), 0)) {
                if ((budget -= 200) < 0) break;
                g64<0>(fc, foff, rws, 0);
                VMW(0);
                PIN(fc);
            }
            if (u == 0 && lane == 0) lds_wr(lds + L_FL + F_OK * 4, 1u);
            TS(12);
        }
        if (role == 1) {
            v4u b0, b1;
            u32 tg = Tq + C_PB1;
            if (first) { wait_eq2(lds + L_FL + F_P * 4, 0, budget); tg = now32() + G_PB0; if ((int)(tg - (t00 + T_PB)) > 0) tg = t00 + T_PB; }
            spin_until(tg);
            TS(7 + 12 * L);
            g128<0>(b0, goff, rws, L * (8 * 16384)); g128<2048>(b1, goff, rws, L * (8 * 16384));
            if (first) { wait_eq2(lds + L_FL + F_W8 * 4, 0, budget); FENCE(); TS(10); }
            v4u W[4];
            {
                const lp w0 = lds + kbase + (4 * c4 + tq) * 64 + ((c4 & 1) * 32) + tp * 8;
                const lp w1 = lds + kbase + (4 * c4 + tq) * 64 + (((c4 & 1) ^ 1) * 32) + tp * 8;
                W[0] = trd2(w0, w0 + 1024); W[1] = trd2(w0 + 2048, w0 + 3072);
                W[2] = trd2(w1, w1 + 1024); W[3] = trd2(w1 + 2048, w1 + 3072);
                FENCE();
            }
            VMW(0);
            PIN(b0); PIN(b1);
            int tries = 0;
            while (__builtin_expect(__any(max(max4(b0), max4(b1)) == SENT), 0)) {
                if ((budget -= 200) < 0) break;
                __builtin_amdgcn_s_sleep(D_R);
                g128<0>(b0, goff, rws, L * (8 * 16384)); g128<2048>(b1, goff, rws, L * (8 * 16384));
                VMW(0);
                PIN(b0); PIN(b1);
                ++tries;
            }
            CNT(30 + L, tries);
            TS(11 + 12 * L);
            float o[4];
#pragma unroll
            for (int i = 0; i < 4; ++i) { float v = asf(b0[i]) + asf(b1[i]); v += dppf<ROR4>(v); v += dppf<ROR8>(v); o[i] = v; }
            const u32 a0 = cvtpk(o[0], o[1]), a1 = cvtpk(o[2], o[3]);
            const v4u Ao = {a0, a1, dppu<SHL2>(a0), dppu<SHL2>(a1)};
            const v4f e0 = mfma(Ao, W[0]), e1 = mfma(Ao, W[1]), e2 = mfma(Ao, W[2]), e3 = mfma(Ao, W[3]);
            const float r0 = e0[0] + e1[1], r1 = e2[0] + e3[1];
            if (lane < 16) { *(LDSP u32*)(lds + L_WP + lane * 32 + u * 4) = asu(r0); *(LDSP u32*)(lds + L_WP + 512 + lane * 32 + u * 4) = asu(r1); }
            FENCE();
            TS(8 + 12 * L);
        } else if (role == 3) {
            v4u W[7];
            if (first) {
                const v4u rw = mkq(A.w2, 1792u * 896u);
                const u32 wvo = (u32)((224 * u + (lane >> 1)) * 896 + ((x - 6) * 14 + s) * 32 + (lane & 1) * 16);
                wait_eq2(lds + L_FL + F_P * 4, 0, budget);
#pragma unroll
                for (int i = 0; i < 7; ++i) dmak(rw, wvo, i * (32 * 896), kbase + i * 1024);
                VMW(0);
            }
            {
                const lp w0 = lds + kbase + (8 * c4 + tq) * 32 + tp * 8;
#pragma unroll
                for (int t = 0; t < 7; ++t) W[t] = trd2(w0 + t * 1024, w0 + t * 1024 + 128);
                FENCE();
            }
            if (first) TS(10);
            spin_until(wait_set(lds + L_FL + (F_TH + L) * 4, budget));
            TS(7 + 12 * L);
            v4u hv;
            int tries = 0;
            for (;;) {
                g128<0>(hv, goff, rws, L * (4 * 4096));
                VMW(0);
                PIN(hv);
                if (__builtin_expect(!__any(max4(hv) == SENT), 1) || (budget -= 200) < 0) break;
                __builtin_amdgcn_s_sleep(D_R);
                ++tries;
            }
            CNT(30 + L, tries);
            TS(11 + 12 * L);
            v4f e[7];
#pragma unroll
            for (int t = 0; t < 7; ++t) e[t] = mfma(hv, W[t]);
            const float ra = (e[0][0] + e[1][1]) + (e[2][2] + e[3][3]), rb = (e[4][0] + e[5][1]) + e[6][2];
            if (lane < 32) *(LDSP u32*)(lds + L_WP + r16 * 64 + (2 * u + c4) * 4) = asu(lane < 16 ? ra : rb);
            FENCE();
            TS(8 + 12 * L);
        }
    }
    DUMP();
}

DEV void aux_role(const Args& A, lp lds, const int w, const int wave) {
    const int lane = lane_id();
    const int x = w & 7, s = w >> 3, a = wave;
    const int r16 = lane & 15, c4 = lane >> 4;
    const v4u rws = mkq(A.ws, 4u << 20);
    const int role = x < 4 ? (s < 28 ? 1 : 0) : (x < 6 ? (s < 28 ? 2 : 0) : (s < 14 ? 3 : 0));
    int budget = 400000;
    const int kq = s >> 2, dq = s & 3, gV = x ^ VX, gQ = x ^ QX, rb = s >> 3, cb = s & 7;
    const int P = (x + 1) & 3, gO = 2 * P + (s >= 14), co = s >= 14 ? s - 14 : s;
    const int c1 = (x - 4) * 28 + s, c2 = (x - 6) * 14 + s;
    const bool valid = r16 < 14;
    int ch = 4 * (valid ? r16 : 13) + c4 + 14 * rb; ch = ch >= 56 ? ch - 56 : ch;
    TS(0);
    const u32 t00 = now32();
    __builtin_amdgcn_s_setprio(3);
    v4u xp; u32 sm0 = 0, sm1 = 0;
    u64 ep;
    asm volatile("s_load_dwordx2 %0, %1, 0x0" : "=s"(ep) : "s"(A.ws) : "memory");
    asm volatile("buffer_load_dwordx4 %0, %1, %2, 0 offen" : "=v"(xp) : "v"(ch * 16), "s"(mkq(A.xin, 896)) : "memory");
    {
        const v4u rq = mkq(A.wq, 448u * 8192u);
        const u32 qo = (u32)((112 * rb + 8 * a + (lane >> 3)) * 8192 + gQ * 1024 + cb * 128 + (((lane & 7) ^ (((c4 & 1) | ((a & 1) << 1)) << 1)) * 16));
        dma(rq, qo, 0, L_Q + a * 1024); dma(rq, qo, 32 * 8192, L_Q + (a + 4) * 1024); dma(rq, qo, 64 * 8192, L_Q + (a + 8) * 1024);
        if (a < 2) dma(rq, qo, 96 * 8192, L_Q + (a + 12) * 1024);
    }
    lds_init(lds, wave * 64 + lane);
    wg_barrier();
    VMW(0);
    PIN(xp);
    asm volatile("s_waitcnt lgkmcnt(0)" : "+s"(ep) :: "memory");
    if (lane == 0) lds_wr(lds + L_FL + (F_W + a) * 4, 0u);
    const u64 tag = ep + 1;
    const u32 tagl = (u32)tag, tagh = (u32)(tag >> 32);
    TS(10);
    if (a == 0 && lane == 0) lds_wr2(lds + L_FL + F_TAG * 4, v2u{tagl, tagh});
    v4u yp;
    lnorm(xp, yp, valid);
    for (;;) { v4u q = lds_rd4(lds + L_FL + F_W * 4); if (rfl(max4(q)) == 0 || --budget <= 0) break; }
    FENCE();
    TS(9);
    const int tq = (lane >> 2) & 3, tp = lane & 3;
    v4u QB[4];
    {
        const lp qb = lds + L_Q + (8 * c4 + tq) * 128 + ((a ^ ((tq >> 1) | ((c4 & 1) << 1))) * 32) + tp * 8;
#pragma unroll
        for (int t = 0; t < 4; ++t) QB[t] = trd2(qb + t * 4096, qb + t * 4096 + 512);
#pragma unroll
        for (int i = 0; i < 4; ++i) QB[3][i] = lane < 32 ? QB[3][i] : 0u;
    }
    const u32 qcol = rb * 512 + cb * 64 + a * 16 + (r16 >> 2) * 4;
    {
        const v4f d0 = mfma(yp, QB[0]), d1 = mfma(yp, QB[1]), d2 = mfma(yp, QB[2]), d3 = mfma(yp, QB[3]);
        const u32 qv = asu((d0[0] + d1[1]) + (d2[2] + d3[3]));
        const u32 off = lane < 16 ? O_A0 + (gQ * 4 + (lane & 3)) * 16384 + (qcol >> 5) * 256 + (qcol & 31) * 4 : OOB;
        TS(1);
        wait_z8(lds + L_FL + F_C * 4, budget);
        s128<0>(v4u{dppu<QB0>(qv), dppu<QB1>(qv), tagl, tagh}, off, rws, 0);
        s128<128>(v4u{dppu<QB2>(qv), dppu<QB3>(qv), tagl, tagh}, off, rws, 0);
        if (lane == 0) lds_wr(lds + L_FL + (F_Q + a) * 4, 0u);
    }
    TS(2);
    SBAR();
    PRIO(0);
    if (a >= 2) {
        const v4u rv = mkq(A.vc, 268435456u);
        const u32 vo = (u32)((gV * 65536 + 61440 + 512 * kq + 256 * (a - 2) + (lane >> 3)) * 512 + dq * 128 + (((lane & 7) ^ (c4 << 1)) * 16));
        const u32 vb = L_V + (a - 2) * 32768;
        const lp fv = lds + L_FL + (F_V + a - 2) * 4;
        wait_z8(lds + L_FL + F_A * 4, budget);
        PRIO(2);
#pragma unroll
        for (int i = N1; i < 8 + QL; ++i) {
            if (i >= N1 + QL) {
                const int rem = (i < 8 ? i : 8) - (i - QL + 1);
                if (rem == 0) VMW(0); else if (rem == 1) VMW(4); else if (rem == 2) VMW(8); else if (rem == 3) VMW(12); else if (rem == 4) VMW(16); else VMW(20);
                if (lane == 0) lds_wr(fv, (u32)(i - QL + 1));
            }
            if (i < 8) {
#pragma unroll
                for (int r = 0; r < 4; ++r) dma(rv, vo, r * 32768 + i * 4096, vb + r * 8192 + i * 1024);
            }
            if (i == 7) TS(11);
        }
        TS(12);
        PRIO(0);
    }
    float bias = 0.f, xres = 0.f;
    u32 Tq = 0;
#pragma nounroll
    for (int L = 0; L < A.nlayers; ++L) {
        const bool first = L == 0;
        if (!first) {
            u32 tx;
            if (a == 3) {
                wait_eq(lds + L_FL + F_OK * 4, 1, budget);
                u32 b;
                for (;;) {
                    g32<0>(b, O_BC + (x * 8 + (s >> 2)) * 128, rws, 0);
                    VMW(0);
                    PIN(b);
                    if (rfl(b) != SENT || (budget -= 200) < 0) break;
                    __builtin_amdgcn_s_sleep(4);
                }
                tx = fixs(rfl(b) + C_X0);
                if (lane == 0) lds_wr(lds + L_FL + F_TB * 4, tx);
                TS(11);
            } else {
                tx = wait_set(lds + L_FL + F_TB * 4, budget);
            }
            spin_until(tx);
            PRIO(3);
            TS(13);
            int tries = 0;
            u32 xt = 0;
            for (;;) {
                g128<0>(xp, O_X2 + (x * 4 + a) * 1024 + ch * 16, rws, 0);
                if (a == 3) g32<0>(xt, O_XT + (x * 4 + (s & 3)) * 128 + (lane < 28 ? lane : 27) * 4, rws, 0);
                VMW(0);
                PIN(xp); PIN(xt);
                if (__builtin_expect(!__any(max(max4(xp), xt) == SENT), 1) || (budget -= 200) < 0) break;
                __builtin_amdgcn_s_sleep(D_R);
                ++tries;
            }
            CNT(27, tries);
            TS(21);
            lnorm(xp, yp, valid);
            const v4f d0 = mfma(yp, QB[0]), d1 = mfma(yp, QB[1]), d2 = mfma(yp, QB[2]), d3 = mfma(yp, QB[3]);
            const u32 qv = asu((d0[0] + d1[1]) + (d2[2] + d3[3]));
            const v4u q4 = {dppu<QB0>(qv), dppu<QB1>(qv), dppu<QB2>(qv), dppu<QB3>(qv)};
            s128<0>(q4, lane < 16 ? O_A1 + (gQ * 4 + (lane & 3)) * 8192 + qcol * 4 : OOB, rws, 0);
            TS(14);
            if (a == 3) {
                const int d = imax_all((int)(xt - tx));
                Tq = fixs(d + D_X <= 0 ? tx : (d + D_X <= 6 ? tx + d + D_X : tx + d + D_XP));
                if (lane == 0) lds_wr(lds + L_FL + F_TX * 4, Tq);
                CNT(26, d);
            }
            PRIO(0);
        }
        if (!first && a != 3) Tq = wait_set(lds + L_FL + F_TX * 4, budget);
        const v4u sv = {SENT, SENT, SENT, SENT};
        if (a < 2) {
            u32 so, mo;
            {
                int ln = lane; PIN(ln);
                const u32 di = 32 * ((ln >> 4) & 1) + 16 * (ln >> 5) + (ln & 15);
                so = first ? ((ln & 1) ? OOB : O_S0 + x * 32768 + a * 16384 + s * 512 + di * 8) : O_S + (8 + x) * 16384 + a * 8192 + s * 256 + di * 4;
                mo = ln < 8 ? (first ? O_ML0 + (x * 8 + ln) * 1024 + s * 32 + a * 16 : O_ML + (64 + x * 8 + ln) * 512 + s * 16 + a * 8) : OOB;
                PIN(so); PIN(mo);
            }
            const lp pp = lds + L_SP + a * 4096 + (c4 >> 1) * 2048 + (c4 & 1) * 256 + r16 * 16;
            if (first) { const lp cp = lds + L_SP + a * 4096 + (opq(lane) & 7) * 528 + ((lane & 1) ^ 1) * 256; while (__any(lds_rd(cp) == SENT) && --budget > 0) {} }
            else spin_until(Tq + C_A1 + P_A1);
            PRIO(3);
            v4u p0, p1, p2, p3;
            do { p0 = lds_rd4(pp); p1 = *(LDSP v4u*)(pp + 512); p2 = *(LDSP v4u*)(pp + 1024); p3 = *(LDSP v4u*)(pp + 1536); }
            while (__any(max(max(max4(p0), max4(p1)), max(max4(p2), max4(p3))) == SENT) && --budget > 0);
            TS(3 + 12 * L);
            const v4f ps = (__builtin_bit_cast(v4f, p0) + __builtin_bit_cast(v4f, p1)) + (__builtin_bit_cast(v4f, p2) + __builtin_bit_cast(v4f, p3));
            const float s0 = tsum32(ps[0], ps[2]), s1 = tsum32(ps[1], ps[3]);
            const u32 sp = cvtpk(s0, s1);
            if (first) s128<0>(v4u{sp, dppu<XOR1>(sp), tagl, tagh}, so, rws, 0); else s32<0>(sp, so, rws, 0);
            const float a0 = blo(sp), a1 = bhi(sp);
            float m = rowmax(fmaxf(a0, a1));
            m = tmax32(tmax16(m));
            float l = ex2((a0 - m) * CE) + ex2((a1 - m) * CE);
            l = rowsum(l);
            l = tsum16(l, l); l = tsum32(l, l);
            if (first) s128<0>(v4u{asu(m), asu(l), tagl, tagh}, mo, rws, 0); else s64<0>(v2u{asu(m), asu(l)}, mo, rws, 0);
            TS(4 + 12 * L);
            if (first) {
                if (lane == 0) lds_wr(lds + L_FL + (F_S + a) * 4, 0u);
                lds_wr4(pp, sv); lds_wr4(pp + 512, sv); lds_wr4(pp + 1024, sv); lds_wr4(pp + 1536, sv);
                if (a == 0 && s == 0) {
                    wait_eq(lds + L_FL + (F_S + 1) * 4, 0, budget);
                    s64<0>(v2u{tagl, tagh}, lane < 16 ? O_F + opq(lane) * 128 + x * 8 : OOB, rws, 0);
                }
                if (role == 1 || role == 2) {
                    const u32 rs_ = role == 1 ? 896u : 3584u;
                    v4u rw;
                    if (role == 1) rw = mkq(A.wo, 4096u * 896u); else rw = mkq(A.w1, 448u * 3584u);
                    const int ln = opq(lane);
                    const u32 wvo_ = (u32)(((role == 1 ? 512 * gO : 0) + 256 * a + (ln >> 2)) * rs_ + (role == 1 ? co : c1) * 64 + (((ln & 3) ^ ((ln >> 3) & 2)) * 16));
                    const int n = (role == 2 && a == 1) ? 12 : 16;
                    const u32 wvo = wvo_, rs = rs_;
                    u32 so = 0, lb = L_K + a * 32768;
                    PRIO(0);
                    { u32 tw = max(now32() - t00 + G_W, (u32)T_W);
                      tw += t00;
                      for (;;) { const lp p = lds + L_FL + F_G * 4; v4u q = lds_rd4(p), r = lds_rd4(p + 16); if (rfl(max(max4(q), max4(r))) == 0 || (int)(now32() - tw) > 0 || --budget <= 0) break; } }
                    PRIO(2);
                    TS(24);
#pragma nounroll
                    for (int i = 0; i < n; ++i) { dmak(rw, wvo, so, lb); so += 16 * rs; lb += (i & 3) == 3 ? 5120 : 1024; }
                    VMW(0);
                    if (lane == 0) lds_wr(lds + L_FL + (F_W8 + a) * 4, 0u);
                    TS(23);
                } else if (W2PF && role == 0 && x >= 6) {
                    const v4u rw = mkq(A.w2, 1792u * 896u);
                    int l = ((s - 14) * 2 + a) * 200 + opq(lane);
                    PRIO(0);
                    wait_z8(lds + L_FL + F_G * 4, budget);
#pragma nounroll
                    for (int i = 0; i < 4; ++i) {
                        const u32 wvo = (lane < 50 && l < 7168) ? (u32)((l >> 2) * 896 + ((x - 6) * 3 + (l & 3)) * 128) : OOB;
                        asm volatile("s_mov_b32 m0, %2\n\ts_nop 0\n\tbuffer_load_dword %0, %1, 0 offen lds" :: "v"(wvo), "s"(rw), "s"(L_PFS) : "memory");
                        l += 50;
                    }
                }
                PRIO(0);
            }
            PRIO(0);
        } else {
            const lp pp = lds + L_PP + (a - 2) * 2048 + (2 * c4) * 256 + r16 * 16;
            if (first) { const lp cp = lds + L_PP + (a - 2) * 2048 + (opq(lane) & 7) * 272; while (__any(lds_rd(cp) == SENT) && --budget > 0) {} }
            else spin_until(Tq + C_S1 + P_S1);
            PRIO(3);
            v4u p0, p1;
            do { p0 = lds_rd4(pp); p1 = *(LDSP v4u*)(pp + 256); } while (__any(max(max4(p0), max4(p1)) == SENT) && --budget > 0);
            TS(3 + 12 * L);
            const float r0 = tsum32(asf(p0.x) + asf(p1.x), asf(p0.z) + asf(p1.z)), r1 = tsum32(asf(p0.y) + asf(p1.y), asf(p0.w) + asf(p1.w));
            const float o = tsum16(r0, r1);
            s32<0>(asu(o), O_PB + gV * 16384 + kq * 2048 + (a - 2) * 1024 + dq * 256 + lane * 4, rws, L * (8 * 16384));
            TS(4 + 12 * L);
            if (first) {
                if (lane == 0) lds_wr(lds + L_FL + (F_P + a - 2) * 4, 0u);
                lds_wr4(pp, sv); lds_wr4(pp + 256, sv);
            }
            PRIO(0);
        }
        if (role == 1 && a == 2) {
            const lp pp = lds + L_WP + (lane & 31) * 32 + (lane >> 5) * 16;
            if (!first) spin_until(Tq + C_PB1 + P_PB1);
            PRIO(3);
            v4u p0;
            do { p0 = lds_rd4(pp); } while (__any(max4(p0) == SENT) && --budget > 0);
            TS(5 + 12 * L);
            float v = (asf(p0.x) + asf(p0.y)) + (asf(p0.z) + asf(p0.w));
            v = tsum32(v, v);
            const u32 vv = asu(v);
            const v4u c = {dppu<QB0>(vv), dppu<QB1>(vv), dppu<QB2>(vv), dppu<QB3>(vv)};
            s128<0>(c, O_PC + ((lane & 3) + 4 * (lane >> 5)) * 16384 + gO * 2048 + ((lane >> 2) & 1) * 1024 + co * 64 + ((lane & 31) >> 3) * 16, rws, L * (8 * 16384));
            if (first) {
                s32<0>(fixs(now32()), lane < 2 ? O_PCT + opq(lane) * 512 + (28 * x + s) * 4 : OOB, rws, 0);
                lds_wr4(pp, sv);
            }
            TS(6 + 12 * L);
            PRIO(0);
        } else if (role == 2) {
            if (first) {
                wait_eq2(lds + L_FL + F_P * 4, 0, budget);
                const u32 tg0 = now32() + G_PC0;
                asm volatile("buffer_load_ushort %0, %1, %2, 0 offen" : "=v"(sm0) : "v"((32 * c1 + 16 * (a & 1) + r16) * 2), "s"(mkq(A.b1, 3584)) : "memory");
                VMW(0);
                PIN(sm0);
                bias = asf(sm0 << 16);
                wait_eq2(lds + L_FL + F_W8 * 4, 0, budget);
                wait_eq(lds + L_FL + F_OK * 4, 1, budget);
                spin_until(tg0);
            } else {
                spin_until(Tq + C_PC1);
            }
            TS(5 + 12 * L);
            PRIO(3);
            const u32 coff = O_PC + (c1 % 7) * 16384 + a * 4096 + ch * 16;
            v4u WB[14];
            v4f t0, t1;
            {
                v4u b0, b1, b2, b3;
                int tries = 0;
                g128<0>(b0, coff, rws, L * (8 * 16384)); g128<1024>(b1, coff, rws, L * (8 * 16384)); g128<2048>(b2, coff, rws, L * (8 * 16384)); g128<3072>(b3, coff, rws, L * (8 * 16384));
                if (a < 2) {
                    int chi = c4 + 14 * rb;
#pragma unroll
                    for (int t = 0; t < 14; ++t) {
                        const lp b = lds + L_K + (chi >> 3) * 8192 + ((chi & 7) * 8 + tq) * 64 + tp * 8;
                        WB[t] = trd2(b + a * 32, b + 256 + (a ^ 1) * 32);
                        chi += 4; chi = chi >= 56 ? chi - 56 : chi;
                    }
                } else {
#pragma unroll
                    for (int t = 0; t < 14; ++t) UNDEF(WB[t]);
                }
                VMW(0);
                PIN(b0); PIN(b1); PIN(b2); PIN(b3);
                while (__builtin_expect(__any(max(max(max4(b0), max4(b1)), max(max4(b2), max4(b3))) == SENT), 0)) {
                    if ((budget -= 200) < 0) break;
                    __builtin_amdgcn_s_sleep(D_R);
                    g128<0>(b0, coff, rws, L * (8 * 16384)); g128<1024>(b1, coff, rws, L * (8 * 16384)); g128<2048>(b2, coff, rws, L * (8 * 16384)); g128<3072>(b3, coff, rws, L * (8 * 16384));
                    VMW(0);
                    PIN(b0); PIN(b1); PIN(b2); PIN(b3);
                    ++tries;
                }
                CNT(30 + L, tries);
                t0 = __builtin_bit_cast(v4f, b0) + __builtin_bit_cast(v4f, b2); t1 = __builtin_bit_cast(v4f, b1) + __builtin_bit_cast(v4f, b3);
            }
            const lp xb = lds + L_V + L * 32768 + lane * 16;
            *(LDSP v4u*)(xb + a * 8192) = __builtin_bit_cast(v4u, t0); *(LDSP v4u*)(xb + a * 8192 + 1024) = __builtin_bit_cast(v4u, t1);
            TS(6 + 12 * L);
            if (a < 2) {
            {
                v4u e0, e1, e2, e3, e4, e5;
                const lp xo = xb + (a ^ 1) * 8192;
                do {
                    e0 = lds_rd4(xo); e1 = *(LDSP v4u*)(xo + 1024); e2 = *(LDSP v4u*)(xb + 16384); e3 = *(LDSP v4u*)(xb + 17408); e4 = *(LDSP v4u*)(xb + 24576); e5 = *(LDSP v4u*)(xb + 25600);
                } while (__any(max(max(max(max4(e0), max4(e1)), max(max4(e2), max4(e3))), max(max4(e4), max4(e5))) == SENT) && --budget > 0);
                t0 = (t0 + __builtin_bit_cast(v4f, e0)) + (__builtin_bit_cast(v4f, e2) + __builtin_bit_cast(v4f, e4));
                t1 = (t1 + __builtin_bit_cast(v4f, e1)) + (__builtin_bit_cast(v4f, e3) + __builtin_bit_cast(v4f, e5));
            }
            TS(9 + 13 * L);
            v4u x1p, y1, y2;
            {
                const u32 a0 = cvtpk(t0[0], t0[1]), a1 = cvtpk(t0[2], t0[3]), a2 = cvtpk(t1[0], t1[1]), a3 = cvtpk(t1[2], t1[3]);
                x1p.x = cvtpk(blo(xp.x) + blo(a0), bhi(xp.x) + bhi(a0)); x1p.y = cvtpk(blo(xp.y) + blo(a1), bhi(xp.y) + bhi(a1));
                x1p.z = cvtpk(blo(xp.z) + blo(a2), bhi(xp.z) + bhi(a2)); x1p.w = cvtpk(blo(xp.w) + blo(a3), bhi(xp.w) + bhi(a3));
            }
            lnorm(x1p, y1, valid);
            lnorm(y1, y2, valid);
            TS(7 + 12 * L);
            v4f e[14];
#pragma unroll
            for (int t = 0; t < 14; ++t) e[t] = mfma(y2, WB[t]);
            const float g0 = (e[0][0] + e[1][1]) + (e[2][2] + e[3][3]), g1 = (e[4][0] + e[5][1]) + (e[6][2] + e[7][3]);
            const float g2 = (e[8][0] + e[9][1]) + (e[10][2] + e[11][3]), g3 = e[12][0] + e[13][1];
            float z = c4 < 2 ? (c4 == 0 ? g0 : g1) : (c4 == 2 ? g2 : g3);
            z = tsum16(z, z); z = tsum32(z, z);
            const float hx = rbf(rbf(z) + bias);
            const float uu = 0.7978845608028654f * (hx + 0.044715f * hx * hx * hx);
            const float hv = hx * __builtin_amdgcn_rcpf(1.f + ex2(uu * (-2.f * 1.4426950408889634f)));
            const u32 pk = cvtpk(hv, dppf<XOR1>(hv));
            s64<0>(v2u{dppu<QB0>(pk), dppu<QB2>(pk)}, lane < 16 ? O_H + (lane & 3) * 4096 + c1 * 64 + a * 32 + (lane >> 2) * 8 : OOB, rws, L * (4 * 4096));
            TS(8 + 12 * L);
            }
            PRIO(0);
        } else if (role == 3 && a == 0) {
            if (first) {
                wait_eq2(lds + L_FL + F_P * 4, 0, budget);
                const u32 tg0 = now32() + G_PC0;
                asm volatile("buffer_load_ushort %0, %1, %2, 0 offen" : "=v"(sm0) : "v"((16 * c2 + r16) * 2), "s"(mkq(A.b2, 896)) : "memory");
                asm volatile("buffer_load_ushort %0, %1, %2, 0 offen" : "=v"(sm1) : "v"((16 * c2 + r16) * 2), "s"(mkq(A.xin, 896)) : "memory");
                VMW(0);
                PIN(sm0); PIN(sm1);
                bias = asf(sm0 << 16); xres = asf(sm1 << 16);
                wait_eq(lds + L_FL + F_OK * 4, 1, budget);
                spin_until(tg0);
                v4u pt;
                for (;;) {
                    g128<0>(pt, O_PCT + (x - 6) * 512 + (lane < 28 ? lane : 27) * 16, rws, 0);
                    VMW(0);
                    PIN(pt);
                    if (!__any(max4(pt) == SENT) || (budget -= 200) < 0) break;
                }
                const int d = imax_all(max(max((int)(pt.x - tg0), (int)(pt.y - tg0)), max((int)(pt.z - tg0), (int)(pt.w - tg0))));
                const u32 tpc = fixs(tg0 + d);
                if (s == 0) { const int i = opq(lane) >> 3; s32<0>(tpc, lane < 32 ? O_BC + ((x == 6 ? (i < 3 ? i : 7) : 3 + i) * 8 + (lane & 7)) * 128 : OOB, rws, 0); }
                if (lane == 0) lds_wr(lds + L_FL + F_TH * 4, fixs(tpc + C_H0));
                CNT(25, tpc);
            } else {
                if (lane == 0) lds_wr(lds + L_FL + (F_TH + 1) * 4, fixs(Tq + C_H1));
                spin_until(Tq + C_PC1);
            }
            TS(5 + 12 * L);
            u32 c0, c1v;
            for (;;) {
                const u32 off = O_PC + 7 * 16384 + (2 * c4) * 2048 + ((r16 >> 2) & 1) * 1024 + c2 * 32 + (r16 >> 3) * 16 + (r16 & 3) * 4;
                g32<0>(c0, off, rws, L * (8 * 16384)); g32<2048>(c1v, off, rws, L * (8 * 16384));
                VMW(0);
                PIN(c0); PIN(c1v);
                if (!__any(max(c0, c1v) == SENT) || (budget -= 200) < 0) break;
                __builtin_amdgcn_s_sleep(D_R);
            }
            float at = asf(c0) + asf(c1v);
            at = tsum16(at, at); at = tsum32(at, at);
            const float x1 = rbf(xres + rbf(at));
            TS(6 + 12 * L);
            const lp pp = lds + L_WP + r16 * 64 + c4 * 16;
            spin_until(rfl(lds_rd(lds + L_FL + (F_TH + L) * 4)) + P_H);
            PRIO(3);
            v4u p0;
            do { p0 = lds_rd4(pp); } while (__any(max4(p0) == SENT) && --budget > 0);
            TS(7 + 12 * L);
            float v = (asf(p0.x) + asf(p0.y)) + (asf(p0.z) + asf(p0.w));
            v = tsum16(v, v); v = tsum32(v, v);
            const float x2 = x1 + rbf(rbf(v) + bias);
            const u32 pk = cvtpk(x2, dppf<XOR1>(x2));
            const v2u o2 = {dppu<QB0>(pk), dppu<QB2>(pk)};
            if (L + 1 < A.nlayers) {
                const u32 off = O_X2 + ((lane & 3) + 4 * c4) * 1024 + c2 * 32 + (r16 >> 2) * 8;
                s64<0>(o2, off, rws, 0); s64<0>(o2, off, rws, 16 * 1024);
                s32<0>(fixs(now32()), lane < 32 ? O_XT + opq(lane) * 128 + c2 * 4 : OOB, rws, 0);
                xres = rbf(x2);
                lds_wr4(pp, sv);
            } else {
                asm volatile("buffer_store_dwordx2 %0, %1, %2, 0 offen\n\ts_nop 1" :: "v"(o2), "v"((lane & 0x33) == 0 ? c2 * 32 + (r16 >> 2) * 8 : OOB), "s"(mkq(A.out, 896)) : "memory");
                if (c2 == 0) s64<0>(v2u{tagl, tagh}, lane == 0 ? O_EPOCH : OOB, rws, 0);
            }
            TS(8 + 12 * L);
            PRIO(0);
        }
    }
    DUMP();
}

extern "C" __global__ void __launch_bounds__(768, 3)
fused_decode(const void* __restrict__ xin, const void* __restrict__ kc, const void* __restrict__ vc, const void* __restrict__ wq,
             const void* __restrict__ wo, const void* __restrict__ w1, const void* __restrict__ b1, const void* __restrict__ w2,
             const void* __restrict__ b2, void* __restrict__ out, void* __restrict__ ws, int nlayers) {
    __shared__ __attribute__((aligned(1024))) char lds_[L_END];
    const lp lds = (lp)lds_;
    const Args A = {xin, kc, vc, wq, wo, w1, b1, w2, b2, out, ws, nlayers};
    const int w = blockIdx.x;
    const int wave = rfl(threadIdx.x >> 6);
    if (wave == 3) {
        u64 pc; u32 n;
        asm volatile(".Lfd_begin:\n\ts_getpc_b64 %0\n\ts_mov_b32 %1, .Lfd_end-.Lfd_begin" : "=s"(pc), "=s"(n));
        const u32 l = threadIdx.x & 63;
        const u32 off = l < 16 ? (((w >> 3) & 15) * 16 + l) * 128 : OOB;
        const v4u rc = mkq((const void*)pc, (int)n > 0 ? n : 0u);
        asm volatile("s_mov_b32 m0, %2\n\ts_nop 0\n\tbuffer_load_dword %0, %1, 0 offen lds" :: "v"(off), "s"(rc), "s"(L_PFS) : "memory");
    }
    if (wave < 4) aux_role(A, lds, w, wave); else kv_role(A, lds, w, wave);
}
extern "C" __global__ void fused_decode_tail() { asm volatile(".Lfd_end:\n\ts_nop 0"); }

extern "C" void kernel_launch(void* const* d_in, const int* in_sizes, int n_in, void* d_out, int out_size, void* d_ws, size_t ws_size, hipStream_t stream) {
    fused_decode<<<256, 768, 0, stream>>>(d_in[0], d_in[1], d_in[2], d_in[3], d_in[4], d_in[6], d_in[7], d_in[8], d_in[9], d_out, d_ws, 2);
}
```
